```python
import torch

def _softmax(x, axis=-1):
    return torch.softmax(x, dim=axis)
def _gelu(x):
    return torch.nn.functional.gelu(x, approximate="tanh")
def _silu(x):
    return torch.nn.functional.silu(x)
def _sigmoid(x):
    return torch.sigmoid(x)
def _layernorm(x, axis=-1, eps=1e-5):
    m = x.mean(dim=axis, keepdim=True)
    v = x.var(dim=axis, keepdim=True, unbiased=False)
    return (x - m) / torch.sqrt(v + eps)
def _scatter_add(tgt, idx, src, axis=0):
    return tgt.clone().index_add_(axis, idx, src)
def _segment_sum(src, seg, n):
    out = torch.zeros((n,) + src.shape[1:], dtype=src.dtype)
    return out.index_add_(0, seg, src)
def _pad(x, pw):
    flat = tuple(v for pair in reversed(pw) for v in pair)
    return torch.nn.functional.pad(x, flat)

kernel_name = 'standalone__gqa_block_cached__near_ridge'

def setup_inputs():

    _w = (lambda a: (lambda t: t.to(torch.bfloat16) if t.is_floating_point() else t)(torch.as_tensor(a)))
    B, Sq, D = 16, 96, 448
    Hq, Hkv, Sk, Dh = 16, 8, 1024, 256
    assert Hq % Hkv == 0, (Hq, Hkv)
    return {
        "x": _w((lambda *s: torch.randn(*s, dtype=torch.bfloat16) * 0.8)(B, Sq, D)),
        "k_cache": _w((lambda *s: torch.randn(*s, dtype=torch.bfloat16) * 0.8)(B, Hkv, Sk, Dh)),
        "v_cache": _w((lambda *s: torch.randn(*s, dtype=torch.bfloat16) * 0.05)(B, Hkv, Sk, Dh)),
        "wq": _w((lambda *s: torch.randn(*s, dtype=torch.bfloat16) * 0.8)(D, Hq * Dh) * (D ** -0.5)),
        "wo": _w((lambda *s: torch.randn(*s, dtype=torch.bfloat16) * 0.05)(Hq * Dh, D) * ((Hq * Dh) ** -0.5)),
    }

def reference(x, k_cache, v_cache, wq, wo):
    B, Sq, D = x.shape
    Hq, Dh = 16, 256
    Hkv = k_cache.shape[1]
    rep = Hq // Hkv
    q = (lambda x, *d: x.permute(*d))((x @ wq).reshape(B, Sq, Hq, Dh), 0, 2, 1, 3)
    k = (lambda x, n, axis: torch.repeat_interleave(x, n, dim=axis))(k_cache, rep, axis=1)
    v = (lambda x, n, axis: torch.repeat_interleave(x, n, dim=axis))(v_cache, rep, axis=1)
    scores = torch.einsum("bhqd,bhkd->bhqk", q, k) * (Dh ** -0.5)
    p = _softmax(scores, axis=-1)
    o = torch.einsum("bhqk,bhkd->bhqd", p, v)
    return (lambda x, *d: x.permute(*d))(o, 0, 2, 1, 3).reshape(B, Sq, Hq * Dh) @ wo
```

```cpp
#include <hip/hip_runtime.h>
#include <hip/hip_bf16.h>
typedef unsigned short u16;
__device__ __forceinline__ float bf2f(u16 v){ return __uint_as_float(((unsigned)v)<<16); }
__device__ __forceinline__ u16 f2bf(float f){ __hip_bfloat16 b = __float2bfloat16(f); return *(u16*)&b; }
constexpr int B=16,SQ=96,D=448,HQ=16,HKV=8,SK=1024,DH=256;
__global__ void gemm_naive(const u16* A,const u16* Bm,u16* C,int M,int N,int K){
  int n = blockIdx.x*blockDim.x+threadIdx.x; int m = blockIdx.y;
  if(n>=N) return;
  float acc=0; for(int k=0;k<K;k++) acc += bf2f(A[(size_t)m*K+k])*bf2f(Bm[(size_t)k*N+n]);
  C[(size_t)m*N+n]=f2bf(acc);
}
__global__ void attn_naive(const u16* Q,const u16* Kc,const u16* Vc,u16* O){
  int q=blockIdx.x, h=blockIdx.y, b=blockIdx.z, t=threadIdx.x;
  __shared__ float qs[DH]; __shared__ float s[SK]; __shared__ float red[256];
  qs[t]=bf2f(Q[((size_t)(b*SQ+q))*HQ*DH + h*DH + t]); __syncthreads();
  const u16* Kb=Kc+((size_t)(b*HKV+h/2))*SK*DH; const u16* Vb=Vc+((size_t)(b*HKV+h/2))*SK*DH;
  float mx=-1e30f;
  for(int k=t;k<SK;k+=256){ float a=0; for(int d=0;d<DH;d++) a+=qs[d]*bf2f(Kb[(size_t)k*DH+d]); a*=0.0625f; s[k]=a; mx=fmaxf(mx,a);}
  red[t]=mx; __syncthreads();
  for(int o=128;o>0;o>>=1){ if(t<o) red[t]=fmaxf(red[t],red[t+o]); __syncthreads(); }
  mx=red[0]; __syncthreads();
  float sm=0; for(int k=t;k<SK;k+=256){ float e=__expf(s[k]-mx); s[k]=e; sm+=e; }
  red[t]=sm; __syncthreads();
  for(int o=128;o>0;o>>=1){ if(t<o) red[t]+=red[t+o]; __syncthreads(); }
  sm=red[0];
  float acc=0; for(int k=0;k<SK;k++) acc+=s[k]*bf2f(Vb[(size_t)k*DH+t]);
  O[((size_t)(b*SQ+q))*HQ*DH + h*DH + t]=f2bf(acc/sm);
}
extern "C" void kernel_launch(void* const* d_in, const int* in_sizes, int n_in, void* d_out, int out_size, void* d_ws, size_t ws_size, hipStream_t stream){
  const u16* x=(const u16*)d_in[0]; const u16* kc=(const u16*)d_in[1]; const u16* vc=(const u16*)d_in[2];
  const u16* wq=(const u16*)d_in[3]; const u16* wo=(const u16*)d_in[4];
  u16* Q=(u16*)d_ws; u16* O=Q+(size_t)B*SQ*HQ*DH;
  gemm_naive<<<dim3(HQ*DH/256,B*SQ),256,0,stream>>>(x,wq,Q,B*SQ,HQ*DH,D);
  attn_naive<<<dim3(SQ,HQ,B),256,0,stream>>>(Q,kc,vc,O);
  gemm_naive<<<dim3((D+255)/256,B*SQ),256,0,stream>>>(O,wo,(u16*)d_out,B*SQ,D,HQ*DH);
}
```

```cpp
#include <hip/hip_runtime.h>
#include <hip/hip_bf16.h>
typedef unsigned short u16;
__device__ __forceinline__ float bf2f(u16 v){ return __uint_as_float(((unsigned)v)<<16); }
__device__ __forceinline__ u16 f2bf(float f){ __hip_bfloat16 b = __float2bfloat16(f); return *(u16*)&b; }
constexpr int B=16,SQ=96,D=448,HQ=16,HKV=8,SK=1024,DH=256;
__global__ void gemm_naive(const u16* A,const u16* Bm,u16* C,int M,int N,int K){
  int n = blockIdx.x*blockDim.x+threadIdx.x; int m = blockIdx.y;
  if(n>=N) return;
  float acc=0; for(int k=0;k<K;k++) acc += bf2f(A[(size_t)m*K+k])*bf2f(Bm[(size_t)k*N+n]);
  C[(size_t)m*N+n]=f2bf(acc);
}
__global__ void attn_naive(const u16* Q,const u16* Kc,const u16* Vc,u16* O){
  int q=blockIdx.x, h=blockIdx.y, b=blockIdx.z, t=threadIdx.x;
  __shared__ float qs[DH]; __shared__ float s[SK]; __shared__ float red[256];
  qs[t]=bf2f(Q[((size_t)(b*SQ+q))*HQ*DH + h*DH + t]); __syncthreads();
  const u16* Kb=Kc+((size_t)(b*HKV+h/2))*SK*DH; const u16* Vb=Vc+((size_t)(b*HKV+h/2))*SK*DH;
  float mx=-1e30f;
  for(int k=t;k<SK;k+=256){ float a=0; for(int d=0;d<DH;d++) a+=qs[d]*bf2f(Kb[(size_t)k*DH+d]); a*=0.0625f; s[k]=a; mx=fmaxf(mx,a);}
  red[t]=mx; __syncthreads();
  for(int o=128;o>0;o>>=1){ if(t<o) red[t]=fmaxf(red[t],red[t+o]); __syncthreads(); }
  mx=red[0]; __syncthreads();
  float sm=0; for(int k=t;k<SK;k+=256){ float e=__expf(s[k]-mx); s[k]=e; sm+=e; }
  red[t]=sm; __syncthreads();
  for(int o=128;o>0;o>>=1){ if(t<o) red[t]+=red[t+o]; __syncthreads(); }
  sm=red[0];
  float acc=0; for(int k=0;k<SK;k++) acc+=s[k]*bf2f(Vb[(size_t)k*DH+t]);
  O[((size_t)(b*SQ+q))*HQ*DH + h*DH + t]=f2bf(acc/sm);
}
#define MULT 1

typedef unsigned u32x4 __attribute__((ext_vector_type(4)));
__global__ __launch_bounds__(1024) void probe(const u32x4* __restrict__ K, const u32x4* __restrict__ V, unsigned* sink){
  unsigned long long t0 = __builtin_amdgcn_s_memrealtime();
  int wg = blockIdx.x; int t = threadIdx.x;
  const u32x4* kp = K + (size_t)wg*16384 + t; const u32x4* vp = V + (size_t)wg*16384 + t;
  u32x4 acc = {0,0,0,0};
  #pragma unroll
  for(int i=0;i<16;i+=4){
    u32x4 a0=kp[(i+0)*1024],a1=kp[(i+1)*1024],a2=kp[(i+2)*1024],a3=kp[(i+3)*1024];
    u32x4 b0=vp[(i+0)*1024],b1=vp[(i+1)*1024],b2=vp[(i+2)*1024],b3=vp[(i+3)*1024];
    acc ^= a0^a1^a2^a3^b0^b1^b2^b3;
  }
  unsigned r = acc[0]^acc[1]^acc[2]^acc[3];
  if(r==0x12345678) sink[t]=r;
  __syncthreads();
  unsigned long long t1 = __builtin_amdgcn_s_memrealtime();
  unsigned long long dur = (t1-t0)*MULT;
  if(dur > 2000000ull) dur = 2000000ull;
  while(__builtin_amdgcn_s_memrealtime()-t0 < dur) __builtin_amdgcn_s_sleep(10);
}
extern "C" void kernel_launch(void* const* d_in, const int* in_sizes, int n_in, void* d_out, int out_size, void* d_ws, size_t ws_size, hipStream_t stream){
  const u16* x=(const u16*)d_in[0]; const u16* kc=(const u16*)d_in[1]; const u16* vc=(const u16*)d_in[2];
  const u16* wq=(const u16*)d_in[3]; const u16* wo=(const u16*)d_in[4];
  u16* Q=(u16*)d_ws; u16* O=Q+(size_t)B*SQ*HQ*DH;
  probe<<<256,1024,0,stream>>>((const u32x4*)kc,(const u32x4*)vc,(unsigned*)((char*)d_ws+(128<<20)));
  gemm_naive<<<dim3(HQ*DH/256,B*SQ),256,0,stream>>>(x,wq,Q,B*SQ,HQ*DH,D);
  attn_naive<<<dim3(SQ,HQ,B),256,0,stream>>>(Q,kc,vc,O);
  gemm_naive<<<dim3((D+255)/256,B*SQ),256,0,stream>>>(O,wo,(u16*)d_out,B*SQ,D,HQ*DH);
}
```

```cpp
#include <hip/hip_runtime.h>
#include <hip/hip_bf16.h>

typedef unsigned short u16;
typedef unsigned u32;
typedef unsigned long long u64;
typedef u32 u32x4 __attribute__((ext_vector_type(4)));
typedef u32 u32x2 __attribute__((ext_vector_type(2)));
typedef u32 u32x8 __attribute__((ext_vector_type(8)));
typedef u32 u32x16 __attribute__((ext_vector_type(16)));
typedef __attribute__((address_space(1))) u32 gu32;
typedef __attribute__((address_space(1))) u16 gu16;
typedef const __attribute__((address_space(1))) u16 gc16;
typedef __attribute__((address_space(1))) u32x2 gu32x2;
typedef float f32x2 __attribute__((ext_vector_type(2)));
typedef __bf16 bf16x2 __attribute__((ext_vector_type(2)));

constexpr int NB = 16, SQ = 96, DM = 448, HQ = 16, HKV = 8, SK = 1024, DH = 256;
constexpr u32 KREG = 0, VREG = 65536, PBUF = 131072, SPARE = 155648, LDS_BYTES = 163840;
constexpr u32 FLAG_OFF = 24u << 20, FROW = 4352, FLAG_BYTES = 32 * FROW;
constexpr u32 PART_BYTES = SQ * DM * 2 + 128;

__device__ __forceinline__ float bf2f(u16 v) { return __uint_as_float(((u32)v) << 16); }
__device__ __forceinline__ u32 pack2(float a, float b) { f32x2 v = {a, b}; return __builtin_bit_cast(u32, __builtin_convertvector(v, bf16x2)); }
__device__ __forceinline__ u16 f2bf(float a) { return (u16)(pack2(a, 0.f) & 0xffffu); }
__device__ __forceinline__ u32x4 srd(const void* p, u32 bytes) {
  const u64 a = (u64)p; u32x4 r = {(u32)a, (u32)(a >> 32) & 0xffffu, bytes, 0x00020000u}; return r;
}

__device__ __forceinline__ void slow_path(char* lds, int tid, gc16* x, gc16* wq, gc16* kc, gc16* vc, gc16* wo, gu16* part) {
  u16* qs = (u16*)lds;
  float* sc = (float*)(lds + 49152);
  float* red = (float*)(lds + 49152 + 4096);
  __syncthreads();
  for (int q = 0; q < SQ; ++q) {
    float a = 0.f;
    for (int d = 0; d < DM; ++d) a += bf2f(x[q * DM + d]) * bf2f(wq[(size_t)d * (HQ * DH) + tid]);
    qs[q * DH + tid] = f2bf(a);
  }
  __syncthreads();
  for (int q = 0; q < SQ; ++q) {
    float mx = -__builtin_inff();
    for (int k = tid; k < SK; k += 256) {
      float a = 0.f;
      for (int d = 0; d < DH; ++d) a += bf2f(qs[q * DH + d]) * bf2f(kc[(size_t)k * DH + d]);
      a *= 0.0625f; sc[k] = a; mx = fmaxf(mx, a);
    }
    red[tid] = mx; __syncthreads();
    for (int o = 128; o > 0; o >>= 1) { if (tid < o) red[tid] = fmaxf(red[tid], red[tid + o]); __syncthreads(); }
    mx = red[0]; __syncthreads();
    float sm = 0.f;
    for (int k = tid; k < SK; k += 256) { const float e = __expf(sc[k] - mx); sc[k] = e; sm += e; }
    red[tid] = sm; __syncthreads();
    for (int o = 128; o > 0; o >>= 1) { if (tid < o) red[tid] += red[tid + o]; __syncthreads(); }
    sm = red[0];
    float a = 0.f;
    for (int k = 0; k < SK; ++k) a += sc[k] * bf2f(vc[(size_t)k * DH + tid]);
    __syncthreads();
    qs[q * DH + tid] = f2bf(a / sm);
  }
  __syncthreads();
  for (int e = tid; e < SQ * DM; e += 256) {
    const int q = e / DM, n = e - q * DM;
    float a = 0.f;
    for (int d = 0; d < DH; ++d) a += bf2f(qs[q * DH + d]) * bf2f(wo[d * DM + n]);
    const int idx = ((((n >> 6) * 4 + ((n >> 4) & 3)) * 3 + (q >> 5)) * 64 + ((n >> 2) & 3) * 16 + (q & 15)) * 8 + ((q >> 4) & 1) * 4 + (n & 3);
    __hip_atomic_store(part + idx, f2bf(a), __ATOMIC_RELAXED, __HIP_MEMORY_SCOPE_AGENT);
  }
}

__device__ __forceinline__ u32 out_off(u32 c) {
  const u32 u = c >> 6, ln = c & 63u, sw = u / 3u, i = u - sw * 3u;
  return ((32 * i + (ln & 15u)) * DM + 16 * sw + 4 * (ln >> 4)) * 2;
}

__global__ __launch_bounds__(256, 1) void gqa_fused(const u16* x, const u16* kc, const u16* vc,
                                                    const u16* wq, const u16* wo, char* ws, u16* out) {
  extern __shared__ __attribute__((aligned(16))) char lds[];
  const u32 lb = (u32)(size_t)lds;
  u32 tid0 = threadIdx.x;
  {
    u32 t0_, t1_;
    asm volatile("s_getpc_b64 s[36:37]\n.Lgqa_pc:\n\ts_lshr_b32 s38, %4, 3\n\tv_lshrrev_b32 %1, 6, %0\n\tv_and_b32 %2, 63, %0\n\t"
                 "v_lshl_add_u32 %1, s38, 2, %1\n\tv_lshl_add_u32 %1, %2, 7, %1\n\tv_lshrrev_b32 %2, 1, %2\n\tv_lshlrev_b32 %1, 7, %1\n\t"
                 "s_mov_b32 m0, %3\n\ts_mov_b32 s38, .Lgqa_exit-.Lgqa_pc\n\ts_mov_b32 s39, 0x20000\n\ts_and_b32 s37, s37, 0xffff\n\t"
                 "v_lshl_add_u32 %1, %2, 25, %1\n\tbuffer_load_dword %1, s[36:39], 0 offen lds"
                 : "+v"(tid0), "=&v"(t0_), "=&v"(t1_) : "s"(lb), "s"(blockIdx.x) : "s36", "s37", "s38", "s39", "scc", "memory");
  }
  u32 l = tid0 & 63u;
  const u32 w = __builtin_amdgcn_readfirstlane(tid0 >> 6);
  const u32 wg = blockIdx.x, kvh = wg & 7u, b = wg >> 4, h = 2u * kvh + ((wg >> 3) & 1u);
  const u16* xb = x + (size_t)b * SQ * DM;
  const u16* wqh = wq + h * DH;
  u32x4 sx = srd(xb, SQ * DM * 2), sq = srd(wqh, DM * HQ * DH * 2 - h * DH * 2);
  u32x4 p0;
#pragma unroll
  for (u32 i = 0; i < 4; ++i)
    p0[i] = (8 * i + (l >> 3)) * 8192 + 128 * w + 16 * ((l & 7u) ^ ((((l >> 4) & 1u) | ((i & 1u) << 1)) << 1)) - 1024 * i;
  const u32 px = (8 * w + (l >> 3)) * 896 + 16 * ((l & 7u) ^ ((4 * w + (l >> 4)) & 7u));
  asm volatile(
    "s_add_u32 m0, s71, 131072\n"
    "s_mov_b32 s74, 0\n"
    "buffer_load_dwordx4 v8, s[40:43], s74 offen offset:0 lds\n"
    "s_add_u32 m0, s71, 135168\n"
    "s_mov_b32 s74, 28672\n"
    "buffer_load_dwordx4 v8, s[40:43], s74 offen offset:0 lds\n"
    "s_add_u32 m0, s71, 139264\n"
    "s_mov_b32 s74, 57344\n"
    "buffer_load_dwordx4 v8, s[40:43], s74 offen offset:0 lds\n"
    "s_add_u32 m0, s65, 0\n"
    "s_mov_b32 s75, 0\n"
    "buffer_load_dwordx4 v0, s[44:47], s75 offen offset:0 lds\n"
    "buffer_load_dwordx4 v1, s[44:47], s75 offen offset:1024 lds\n"
    "buffer_load_dwordx4 v2, s[44:47], s75 offen offset:2048 lds\n"
    "buffer_load_dwordx4 v3, s[44:47], s75 offen offset:3072 lds\n"
    "s_add_u32 m0, s71, 155648\n"
    "s_mov_b32 s74, 128\n"
    "buffer_load_dwordx4 v8, s[40:43], s74 offen offset:0 lds\n"
    "s_add_u32 m0, s71, 159744\n"
    "s_mov_b32 s74, 28800\n"
    "buffer_load_dwordx4 v8, s[40:43], s74 offen offset:0 lds\n"
    "s_add_u32 m0, s71, 61440\n"
    "s_mov_b32 s74, 57472\n"
    "buffer_load_dwordx4 v8, s[40:43], s74 offen offset:0 lds\n"
    "s_add_u32 m0, s65, 4096\n"
    "s_mov_b32 s75, 262144\n"
    "buffer_load_dwordx4 v0, s[44:47], s75 offen offset:0 lds\n"
    "buffer_load_dwordx4 v1, s[44:47], s75 offen offset:1024 lds\n"
    "buffer_load_dwordx4 v2, s[44:47], s75 offen offset:2048 lds\n"
    "buffer_load_dwordx4 v3, s[44:47], s75 offen offset:3072 lds\n"
    : "+v"(l)
    : "{v[0:3]}"(p0), "{v8}"(px), "{s[40:43]}"(sx), "{s[44:47]}"(sq), "{s65}"(lb + VREG + w * 16384), "{s71}"(lb + w * 1024)
    : "s74", "s75", "scc", "memory");
  const u32 tid = w * 64 + l;
  const u32 lr = l >> 3, lc = l & 7u, r = l & 15u, g = l >> 4, q4 = r >> 2, p = l & 3u;
  const u16* kb = kc + (size_t)(b * HKV + kvh) * SK * DH;
  const u16* vb = vc + (size_t)(b * HKV + kvh) * SK * DH;
  const u16* woh = wo + (size_t)h * DH * DM;
  char* part = ws + (size_t)(b * HQ + h) * PART_BYTES;

  u32x16 pa, pb; u32x8 pc;
#pragma unroll
  for (u32 i = 0; i < 4; ++i) {
    pa[i] = p0[i];
    pa[4 + i] = lb + VREG + w * 16384 + (8 * g + q4) * 128 + 32 * (i ^ (((q4 >> 1) & 1u) | ((g & 1u) << 1))) + 8 * p;
    pb[i] = (32 * w + 8 * i + lr) * 512 + 16 * (lc ^ ((4 * i + (lr >> 1)) & 7u)) - 1024 * i;
    pb[4 + i] = (8 * i + lr) * 512 + 128 * w + 16 * (lc ^ (((lr >> 1) & 3u) << 1)) - 1024 * i;
    pb[10 + i] = lb + VREG + w * 16384 + (4 * g + q4) * 128 + 32 * (i ^ ((2 * g + (q4 >> 1)) & 3u)) + 8 * p;
  }
  pa[8] = px;
  pa[10] = l;
#pragma unroll
  for (u32 s = 0; s < 2; ++s) {
    const u32 a = r * 128 + 16 * ((4 * s + g) ^ ((r >> 1) & 7u));
    pa[11 + s] = lb + a; pa[13 + s] = lb + PBUF + a; pb[8 + s] = lb + KREG + w * 16384 + a;
  }
  pa[15] = lb + VREG + w * 16384 + 16 * (16 * (g >> 1) + r) + 8 * (g & 1u);
  pa[9] = lb + PBUF + w * 8192 + 16 * (16 * (g >> 1) + r) + 8 * (g & 1u);
  pb[14] = lb + PBUF + w * 6144 + l * 16;
  pb[15] = lb + PBUF + l * 16;
  pc[0] = lb + VREG + l * 16;
  pc[1] = l;
  pc[2] = lb + (4 * g + q4) * 128 + 32 * (w ^ ((2 * g + (q4 >> 1)) & 3u)) + 8 * p;
  pc[3] = l;
  pc[4] = lb + SPARE + r * 16 + w * 4;
  pc[5] = lb + SPARE + r * 16;
  pc[6] = lb + SPARE + r * 80 + (4 * w + g) * 4;
  pc[7] = lb + SPARE + r * 80 + g * 16;
  u32 pd = l * 128;
  const u32 hl = (wg >> 3) & 1u;
  const u32 wq_t = (l >> 2) * 64 + b * 4 + w, x1 = (hl * 4 + w) * 24 + l, x2 = (hl * 4 + w) * 36 + l;
  u32x4 pe = {b * FROW + r * 4, tid < 32 ? (16 + b) * FROW + h * 256 + l * 4 : 0x7ffffff0u,
              l < 12 ? (64 + wq_t) * 8192 + (l & 3u) * 128 : 0x7ffffff0u, l < 24 ? (x1 >> 1) * 896 + (2 + (x1 & 1u)) * 128 : 0x7ffffff0u};
  u32x4 pf = {l < 12 ? (256 + wq_t) * 8192 + (l & 3u) * 128 : 0x7ffffff0u, l < 36 ? (x2 / 3u) * 896 + (4 + x2 % 3u) * 128 : 0x7ffffff0u,
              l < 1 ? l * 65536 : 0x7ffffff0u, l};
  u32 vn;
  u32x4 sk = srd(kb, SK * DH * 2), sv = srd(vb, SK * DH * 2),
        so = srd(woh, DH * DM * 2), sp = srd(part, PART_BYTES);
  const u32 t0 = ((b) & 7u) * 65536;
  u32x8 sa = {lb + KREG + w * 16384, lb + VREG + w * 16384, t0, t0, 128u, 0u, __float_as_uint(0.09016844005556021f), lb + w * 1024};
  u32x4 sb = {(w & 1u) * 4 * 28672 + 128 * (w >> 1), 0x5f800000u  , w * 2 * 28672, lb + w * 8192};
  const u32 tstep = (((1u) & 7u) | 1u) * 65536;
  u32x8 sc = {tstep - 512, tstep - 65536, (b * 4 + w) * 3584, w * 128, w * 16384, w * 3072 - (lb + VREG), h, (16 + b) * FROW};
  const u32x4 sf = srd(ws + FLAG_OFF, FLAG_BYTES);
  u32 sn;
  u64 bad;
  asm volatile(
    "s_mov_b64 s[72:73], 0\n"
    "s_mov_b32 s88, 0\n"
    "s_mov_b32 s88, 0\n"
    "buffer_load_dword v225, v18, s[44:47], s88 offen offset:0\n"
    "buffer_load_dword v225, v19, s[40:43], s88 offen offset:0\n"
    "s_add_u32 m0, s65, 8192\n"
    "s_mov_b32 s75, 524288\n"
    "buffer_load_dwordx4 v0, s[44:47], s75 offen offset:0 lds\n"
    "buffer_load_dwordx4 v1, s[44:47], s75 offen offset:1024 lds\n"
    "buffer_load_dwordx4 v2, s[44:47], s75 offen offset:2048 lds\n"
    "buffer_load_dwordx4 v3, s[44:47], s75 offen offset:3072 lds\n"
    "s_waitcnt vmcnt(17)\n"
    "s_barrier\n"
    "s_waitcnt vmcnt(13)\n"
    "ds_read_b64_tr_b16 v[168:169], v4 offset:0\n"
    "ds_read_b64_tr_b16 v[170:171], v4 offset:512\n"
    "ds_read_b64_tr_b16 v[172:173], v5 offset:0\n"
    "ds_read_b64_tr_b16 v[174:175], v5 offset:512\n"
    "ds_read_b64_tr_b16 v[176:177], v6 offset:0\n"
    "ds_read_b64_tr_b16 v[178:179], v6 offset:512\n"
    "ds_read_b64_tr_b16 v[180:181], v7 offset:0\n"
    "ds_read_b64_tr_b16 v[182:183], v7 offset:512\n"
    "ds_read_b128 a[208:211], v13 offset:0\n"
    "ds_read_b128 a[212:215], v13 offset:2048\n"
    "ds_read_b128 a[216:219], v13 offset:4096\n"
    "ds_read_b128 a[220:223], v13 offset:6144\n"
    "ds_read_b128 a[224:227], v13 offset:8192\n"
    "ds_read_b128 a[228:231], v13 offset:10240\n"
    "s_waitcnt lgkmcnt(5)\n"
    "v_mfma_f32_16x16x32_bf16 v[48:51], v[168:171], a[208:211], 0\n"
    "v_mfma_f32_16x16x32_bf16 v[52:55], v[172:175], a[208:211], 0\n"
    "s_add_u32 m0, s71, 143360\n"
    "s_mov_b32 s74, 256\n"
    "s_waitcnt vmcnt(6)\n"
    "ds_read_b64_tr_b16 v[184:185], v4 offset:4096\n"
    "v_mfma_f32_16x16x32_bf16 v[56:59], v[176:179], a[208:211], 0\n"
    "buffer_load_dwordx4 v8, s[40:43], s74 offen offset:0 lds\n"
    "v_mfma_f32_16x16x32_bf16 v[60:63], v[180:183], a[208:211], 0\n"
    "s_waitcnt lgkmcnt(5)\n"
    "s_add_u32 m0, s71, 147456\n"
    "s_mov_b32 s74, 28928\n"
    "ds_read_b64_tr_b16 v[186:187], v4 offset:4608\n"
    "ds_read_b64_tr_b16 v[188:189], v5 offset:4096\n"
    "v_mfma_f32_16x16x32_bf16 v[64:67], v[168:171], a[212:215], 0\n"
    "buffer_load_dwordx4 v8, s[40:43], s74 offen offset:0 lds\n"
    "v_mfma_f32_16x16x32_bf16 v[68:71], v[172:175], a[212:215], 0\n"
    "s_add_u32 m0, s71, 151552\n"
    "s_mov_b32 s74, 57600\n"
    "ds_read_b64_tr_b16 v[190:191], v5 offset:4608\n"
    "ds_read_b64_tr_b16 v[192:193], v6 offset:4096\n"
    "v_mfma_f32_16x16x32_bf16 v[72:75], v[176:179], a[212:215], 0\n"
    "buffer_load_dwordx4 v8, s[40:43], s74 offen offset:0 lds\n"
    "v_mfma_f32_16x16x32_bf16 v[76:79], v[180:183], a[212:215], 0\n"
    "s_waitcnt lgkmcnt(8)\n"
    "s_add_u32 m0, s65, 12288\n"
    "s_mov_b32 s75, 786432\n"
    "ds_read_b64_tr_b16 v[194:195], v6 offset:4608\n"
    "ds_read_b64_tr_b16 v[196:197], v7 offset:4096\n"
    "v_mfma_f32_16x16x32_bf16 v[80:83], v[168:171], a[216:219], 0\n"
    "buffer_load_dwordx4 v0, s[44:47], s75 offen offset:0 lds\n"
    "v_mfma_f32_16x16x32_bf16 v[84:87], v[172:175], a[216:219], 0\n"
    "ds_read_b64_tr_b16 v[198:199], v7 offset:4608\n"
    "v_mfma_f32_16x16x32_bf16 v[88:91], v[176:179], a[216:219], 0\n"
    "ds_read_b128 a[232:235], v14 offset:0\n"
    "v_mfma_f32_16x16x32_bf16 v[92:95], v[180:183], a[216:219], 0\n"
    "s_waitcnt lgkmcnt(11)\n"
    "buffer_load_dwordx4 v1, s[44:47], s75 offen offset:1024 lds\n"
    "v_mfma_f32_16x16x32_bf16 v[96:99], v[168:171], a[220:223], 0\n"
    "ds_read_b128 a[236:239], v14 offset:2048\n"
    "ds_read_b128 a[240:243], v14 offset:4096\n"
    "v_mfma_f32_16x16x32_bf16 v[100:103], v[172:175], a[220:223], 0\n"
    "buffer_load_dwordx4 v2, s[44:47], s75 offen offset:2048 lds\n"
    "v_mfma_f32_16x16x32_bf16 v[104:107], v[176:179], a[220:223], 0\n"
    "ds_read_b128 a[244:247], v14 offset:6144\n"
    "ds_read_b128 a[248:251], v14 offset:8192\n"
    "v_mfma_f32_16x16x32_bf16 v[108:111], v[180:183], a[220:223], 0\n"
    "s_waitcnt lgkmcnt(14)\n"
    "buffer_load_dwordx4 v3, s[44:47], s75 offen offset:3072 lds\n"
    "v_mfma_f32_16x16x32_bf16 v[112:115], v[168:171], a[224:227], 0\n"
    "s_add_u32 m0, s65, 0\n"
    "s_mov_b32 s75, 1048576\n"
    "ds_read_b128 a[252:255], v14 offset:10240\n"
    "v_mfma_f32_16x16x32_bf16 v[116:119], v[172:175], a[224:227], 0\n"
    "buffer_load_dwordx4 v0, s[44:47], s75 offen offset:0 lds\n"
    "v_mfma_f32_16x16x32_bf16 v[120:123], v[176:179], a[224:227], 0\n"
    "buffer_load_dwordx4 v1, s[44:47], s75 offen offset:1024 lds\n"
    "v_mfma_f32_16x16x32_bf16 v[124:127], v[180:183], a[224:227], 0\n"
    "s_waitcnt lgkmcnt(14)\n"
    "buffer_load_dwordx4 v2, s[44:47], s75 offen offset:2048 lds\n"
    "v_mfma_f32_16x16x32_bf16 v[128:131], v[168:171], a[228:231], 0\n"
    "v_mfma_f32_16x16x32_bf16 v[132:135], v[172:175], a[228:231], 0\n"
    "buffer_load_dwordx4 v3, s[44:47], s75 offen offset:3072 lds\n"
    "v_mfma_f32_16x16x32_bf16 v[136:139], v[176:179], a[228:231], 0\n"
    "v_mfma_f32_16x16x32_bf16 v[140:143], v[180:183], a[228:231], 0\n"
    "s_waitcnt lgkmcnt(5)\n"
    "v_mfma_f32_16x16x32_bf16 v[48:51], v[184:187], a[232:235], v[48:51]\n"
    "v_mfma_f32_16x16x32_bf16 v[52:55], v[188:191], a[232:235], v[52:55]\n"
    "s_waitcnt vmcnt(11)\n"
    "ds_read_b64_tr_b16 v[168:169], v4 offset:8192\n"
    "v_mfma_f32_16x16x32_bf16 v[56:59], v[192:195], a[232:235], v[56:59]\n"
    "s_waitcnt lgkmcnt(1)\n"
    "s_barrier\n"
    "ds_read_b64_tr_b16 v[170:171], v4 offset:8704\n"
    "v_mfma_f32_16x16x32_bf16 v[60:63], v[196:199], a[232:235], v[60:63]\n"
    "s_add_u32 m0, s65, 4096\n"
    "s_mov_b32 s75, 1310720\n"
    "ds_read_b64_tr_b16 v[172:173], v5 offset:8192\n"
    "v_mfma_f32_16x16x32_bf16 v[64:67], v[184:187], a[236:239], v[64:67]\n"
    "buffer_load_dwordx4 v0, s[44:47], s75 offen offset:0 lds\n"
    "v_mfma_f32_16x16x32_bf16 v[68:71], v[188:191], a[236:239], v[68:71]\n"
    "ds_read_b64_tr_b16 v[174:175], v5 offset:8704\n"
    "ds_read_b64_tr_b16 v[176:177], v6 offset:8192\n"
    "v_mfma_f32_16x16x32_bf16 v[72:75], v[192:195], a[236:239], v[72:75]\n"
    "buffer_load_dwordx4 v1, s[44:47], s75 offen offset:1024 lds\n"
    "v_mfma_f32_16x16x32_bf16 v[76:79], v[196:199], a[236:239], v[76:79]\n"
    "ds_read_b64_tr_b16 v[178:179], v6 offset:8704\n"
    "ds_read_b64_tr_b16 v[180:181], v7 offset:8192\n"
    "v_mfma_f32_16x16x32_bf16 v[80:83], v[184:187], a[240:243], v[80:83]\n"
    "ds_read_b64_tr_b16 v[182:183], v7 offset:8704\n"
    "v_mfma_f32_16x16x32_bf16 v[84:87], v[188:191], a[240:243], v[84:87]\n"
    "buffer_load_dwordx4 v2, s[44:47], s75 offen offset:2048 lds\n"
    "v_mfma_f32_16x16x32_bf16 v[88:91], v[192:195], a[240:243], v[88:91]\n"
    "ds_read_b128 a[208:211], v13 offset:24576\n"
    "v_mfma_f32_16x16x32_bf16 v[92:95], v[196:199], a[240:243], v[92:95]\n"
    "ds_read_b128 a[212:215], v13 offset:26624\n"
    "v_mfma_f32_16x16x32_bf16 v[96:99], v[184:187], a[244:247], v[96:99]\n"
    "ds_read_b128 a[216:219], v13 offset:28672\n"
    "v_mfma_f32_16x16x32_bf16 v[100:103], v[188:191], a[244:247], v[100:103]\n"
    "buffer_load_dwordx4 v3, s[44:47], s75 offen offset:3072 lds\n"
    "v_mfma_f32_16x16x32_bf16 v[104:107], v[192:195], a[244:247], v[104:107]\n"
    "ds_read_b128 a[220:223], v13 offset:30720\n"
    "ds_read_b128 a[224:227], v11 offset:61440\n"
    "v_mfma_f32_16x16x32_bf16 v[108:111], v[196:199], a[244:247], v[108:111]\n"
    "s_add_u32 m0, s71, 131072\n"
    "s_mov_b32 s74, 384\n"
    "ds_read_b128 a[228:231], v11 offset:63488\n"
    "v_mfma_f32_16x16x32_bf16 v[112:115], v[184:187], a[248:251], v[112:115]\n"
    "buffer_load_dwordx4 v8, s[40:43], s74 offen offset:0 lds\n"
    "s_add_u32 m0, s71, 135168\n"
    "s_mov_b32 s74, 29056\n"
    "v_mfma_f32_16x16x32_bf16 v[116:119], v[188:191], a[248:251], v[116:119]\n"
    "buffer_load_dwordx4 v8, s[40:43], s74 offen offset:0 lds\n"
    "v_mfma_f32_16x16x32_bf16 v[120:123], v[192:195], a[248:251], v[120:123]\n"
    "v_mfma_f32_16x16x32_bf16 v[124:127], v[196:199], a[248:251], v[124:127]\n"
    "s_add_u32 m0, s71, 139264\n"
    "s_mov_b32 s74, 57728\n"
    "v_mfma_f32_16x16x32_bf16 v[128:131], v[184:187], a[252:255], v[128:131]\n"
    "buffer_load_dwordx4 v8, s[40:43], s74 offen offset:0 lds\n"
    "v_mfma_f32_16x16x32_bf16 v[132:135], v[188:191], a[252:255], v[132:135]\n"
    "v_mfma_f32_16x16x32_bf16 v[136:139], v[192:195], a[252:255], v[136:139]\n"
    "v_mfma_f32_16x16x32_bf16 v[140:143], v[196:199], a[252:255], v[140:143]\n"
    "s_waitcnt lgkmcnt(5)\n"
    "v_mfma_f32_16x16x32_bf16 v[48:51], v[168:171], a[208:211], v[48:51]\n"
    "v_mfma_f32_16x16x32_bf16 v[52:55], v[172:175], a[208:211], v[52:55]\n"
    "s_add_u32 m0, s65, 8192\n"
    "s_mov_b32 s75, 1572864\n"
    "s_waitcnt vmcnt(11)\n"
    "ds_read_b64_tr_b16 v[184:185], v4 offset:12288\n"
    "v_mfma_f32_16x16x32_bf16 v[56:59], v[176:179], a[208:211], v[56:59]\n"
    "buffer_load_dwordx4 v0, s[44:47], s75 offen offset:0 lds\n"
    "v_mfma_f32_16x16x32_bf16 v[60:63], v[180:183], a[208:211], v[60:63]\n"
    "s_waitcnt lgkmcnt(5)\n"
    "ds_read_b64_tr_b16 v[186:187], v4 offset:12800\n"
    "ds_read_b64_tr_b16 v[188:189], v5 offset:12288\n"
    "v_mfma_f32_16x16x32_bf16 v[64:67], v[168:171], a[212:215], v[64:67]\n"
    "ds_read_b64_tr_b16 v[190:191], v5 offset:12800\n"
    "v_mfma_f32_16x16x32_bf16 v[68:71], v[172:175], a[212:215], v[68:71]\n"
    "buffer_load_dwordx4 v1, s[44:47], s75 offen offset:1024 lds\n"
    "v_mfma_f32_16x16x32_bf16 v[72:75], v[176:179], a[212:215], v[72:75]\n"
    "ds_read_b64_tr_b16 v[192:193], v6 offset:12288\n"
    "ds_read_b64_tr_b16 v[194:195], v6 offset:12800\n"
    "v_mfma_f32_16x16x32_bf16 v[76:79], v[180:183], a[212:215], v[76:79]\n"
    "s_waitcnt lgkmcnt(9)\n"
    "ds_read_b64_tr_b16 v[196:197], v7 offset:12288\n"
    "v_mfma_f32_16x16x32_bf16 v[80:83], v[168:171], a[216:219], v[80:83]\n"
    "buffer_load_dwordx4 v2, s[44:47], s75 offen offset:2048 lds\n"
    "v_mfma_f32_16x16x32_bf16 v[84:87], v[172:175], a[216:219], v[84:87]\n"
    "ds_read_b64_tr_b16 v[198:199], v7 offset:12800\n"
    "v_mfma_f32_16x16x32_bf16 v[88:91], v[176:179], a[216:219], v[88:91]\n"
    "ds_read_b128 a[232:235], v14 offset:24576\n"
    "v_mfma_f32_16x16x32_bf16 v[92:95], v[180:183], a[216:219], v[92:95]\n"
    "s_waitcnt lgkmcnt(11)\n"
    "ds_read_b128 a[236:239], v14 offset:26624\n"
    "v_mfma_f32_16x16x32_bf16 v[96:99], v[168:171], a[220:223], v[96:99]\n"
    "buffer_load_dwordx4 v3, s[44:47], s75 offen offset:3072 lds\n"
    "v_mfma_f32_16x16x32_bf16 v[100:103], v[172:175], a[220:223], v[100:103]\n"
    "ds_read_b128 a[240:243], v14 offset:28672\n"
    "ds_read_b128 a[244:247], v14 offset:30720\n"
    "v_mfma_f32_16x16x32_bf16 v[104:107], v[176:179], a[220:223], v[104:107]\n"
    "s_mov_b32 s88, 0\n"
    "ds_read_b128 a[248:251], v12 offset:61440\n"
    "v_mfma_f32_16x16x32_bf16 v[108:111], v[180:183], a[220:223], v[108:111]\n"
    "s_waitcnt lgkmcnt(14)\n"
    "buffer_load_dword v225, v144, s[44:47], s88 offen offset:0\n"
    "v_mfma_f32_16x16x32_bf16 v[112:115], v[168:171], a[224:227], v[112:115]\n"
    "ds_read_b128 a[252:255], v12 offset:63488\n"
    "v_mfma_f32_16x16x32_bf16 v[116:119], v[172:175], a[224:227], v[116:119]\n"
    "buffer_load_dword v225, v145, s[40:43], s88 offen offset:0\n"
    "v_mfma_f32_16x16x32_bf16 v[120:123], v[176:179], a[224:227], v[120:123]\n"
    "v_mfma_f32_16x16x32_bf16 v[124:127], v[180:183], a[224:227], v[124:127]\n"
    "s_waitcnt lgkmcnt(14)\n"
    "buffer_load_dword v207, v16, s[96:99], 0 offen sc1\n"
    "v_mfma_f32_16x16x32_bf16 v[128:131], v[168:171], a[228:231], v[128:131]\n"
    "v_mfma_f32_16x16x32_bf16 v[132:135], v[172:175], a[228:231], v[132:135]\n"
    "v_mfma_f32_16x16x32_bf16 v[136:139], v[176:179], a[228:231], v[136:139]\n"
    "v_mfma_f32_16x16x32_bf16 v[140:143], v[180:183], a[228:231], v[140:143]\n"
    "s_waitcnt lgkmcnt(5)\n"
    "v_mfma_f32_16x16x32_bf16 v[48:51], v[184:187], a[232:235], v[48:51]\n"
    "v_mfma_f32_16x16x32_bf16 v[52:55], v[188:191], a[232:235], v[52:55]\n"
    "s_waitcnt vmcnt(14)\n"
    "ds_read_b64_tr_b16 v[168:169], v4 offset:0\n"
    "v_mfma_f32_16x16x32_bf16 v[56:59], v[192:195], a[232:235], v[56:59]\n"
    "s_waitcnt lgkmcnt(1)\n"
    "s_barrier\n"
    "ds_read_b64_tr_b16 v[170:171], v4 offset:512\n"
    "v_mfma_f32_16x16x32_bf16 v[60:63], v[196:199], a[232:235], v[60:63]\n"
    "s_add_u32 m0, s65, 12288\n"
    "s_mov_b32 s75, 1835008\n"
    "ds_read_b64_tr_b16 v[172:173], v5 offset:0\n"
    "v_mfma_f32_16x16x32_bf16 v[64:67], v[184:187], a[236:239], v[64:67]\n"
    "buffer_load_dwordx4 v0, s[44:47], s75 offen offset:0 lds\n"
    "v_mfma_f32_16x16x32_bf16 v[68:71], v[188:191], a[236:239], v[68:71]\n"
    "ds_read_b64_tr_b16 v[174:175], v5 offset:512\n"
    "ds_read_b64_tr_b16 v[176:177], v6 offset:0\n"
    "v_mfma_f32_16x16x32_bf16 v[72:75], v[192:195], a[236:239], v[72:75]\n"
    "buffer_load_dwordx4 v1, s[44:47], s75 offen offset:1024 lds\n"
    "v_mfma_f32_16x16x32_bf16 v[76:79], v[196:199], a[236:239], v[76:79]\n"
    "ds_read_b64_tr_b16 v[178:179], v6 offset:512\n"
    "ds_read_b64_tr_b16 v[180:181], v7 offset:0\n"
    "v_mfma_f32_16x16x32_bf16 v[80:83], v[184:187], a[240:243], v[80:83]\n"
    "ds_read_b64_tr_b16 v[182:183], v7 offset:512\n"
    "v_mfma_f32_16x16x32_bf16 v[84:87], v[188:191], a[240:243], v[84:87]\n"
    "buffer_load_dwordx4 v2, s[44:47], s75 offen offset:2048 lds\n"
    "v_mfma_f32_16x16x32_bf16 v[88:91], v[192:195], a[240:243], v[88:91]\n"
    "ds_read_b128 a[208:211], v13 offset:12288\n"
    "v_mfma_f32_16x16x32_bf16 v[92:95], v[196:199], a[240:243], v[92:95]\n"
    "ds_read_b128 a[212:215], v13 offset:14336\n"
    "v_mfma_f32_16x16x32_bf16 v[96:99], v[184:187], a[244:247], v[96:99]\n"
    "ds_read_b128 a[216:219], v13 offset:16384\n"
    "v_mfma_f32_16x16x32_bf16 v[100:103], v[188:191], a[244:247], v[100:103]\n"
    "buffer_load_dwordx4 v3, s[44:47], s75 offen offset:3072 lds\n"
    "v_mfma_f32_16x16x32_bf16 v[104:107], v[192:195], a[244:247], v[104:107]\n"
    "ds_read_b128 a[220:223], v13 offset:18432\n"
    "ds_read_b128 a[224:227], v13 offset:20480\n"
    "v_mfma_f32_16x16x32_bf16 v[108:111], v[196:199], a[244:247], v[108:111]\n"
    "s_add_u32 m0, s71, 155648\n"
    "s_mov_b32 s74, 512\n"
    "ds_read_b128 a[228:231], v13 offset:22528\n"
    "v_mfma_f32_16x16x32_bf16 v[112:115], v[184:187], a[248:251], v[112:115]\n"
    "buffer_load_dwordx4 v8, s[40:43], s74 offen offset:0 lds\n"
    "s_add_u32 m0, s71, 159744\n"
    "s_mov_b32 s74, 29184\n"
    "v_mfma_f32_16x16x32_bf16 v[116:119], v[188:191], a[248:251], v[116:119]\n"
    "buffer_load_dwordx4 v8, s[40:43], s74 offen offset:0 lds\n"
    "v_mfma_f32_16x16x32_bf16 v[120:123], v[192:195], a[248:251], v[120:123]\n"
    "v_mfma_f32_16x16x32_bf16 v[124:127], v[196:199], a[248:251], v[124:127]\n"
    "s_add_u32 m0, s71, 61440\n"
    "s_mov_b32 s74, 57856\n"
    "v_mfma_f32_16x16x32_bf16 v[128:131], v[184:187], a[252:255], v[128:131]\n"
    "buffer_load_dwordx4 v8, s[40:43], s74 offen offset:0 lds\n"
    "v_mfma_f32_16x16x32_bf16 v[132:135], v[188:191], a[252:255], v[132:135]\n"
    "v_mfma_f32_16x16x32_bf16 v[136:139], v[192:195], a[252:255], v[136:139]\n"
    "v_mfma_f32_16x16x32_bf16 v[140:143], v[196:199], a[252:255], v[140:143]\n"
    "s_waitcnt lgkmcnt(5)\n"
    "v_mfma_f32_16x16x32_bf16 v[48:51], v[168:171], a[208:211], v[48:51]\n"
    "v_mfma_f32_16x16x32_bf16 v[52:55], v[172:175], a[208:211], v[52:55]\n"
    "s_add_u32 m0, s65, 0\n"
    "s_mov_b32 s75, 2097152\n"
    "s_waitcnt vmcnt(17)\n"
    "ds_read_b64_tr_b16 v[184:185], v4 offset:4096\n"
    "v_mfma_f32_16x16x32_bf16 v[56:59], v[176:179], a[208:211], v[56:59]\n"
    "buffer_load_dwordx4 v0, s[44:47], s75 offen offset:0 lds\n"
    "v_mfma_f32_16x16x32_bf16 v[60:63], v[180:183], a[208:211], v[60:63]\n"
    "s_waitcnt lgkmcnt(5)\n"
    "ds_read_b64_tr_b16 v[186:187], v4 offset:4608\n"
    "ds_read_b64_tr_b16 v[188:189], v5 offset:4096\n"
    "v_mfma_f32_16x16x32_bf16 v[64:67], v[168:171], a[212:215], v[64:67]\n"
    "ds_read_b64_tr_b16 v[190:191], v5 offset:4608\n"
    "v_mfma_f32_16x16x32_bf16 v[68:71], v[172:175], a[212:215], v[68:71]\n"
    "ds_read_b64_tr_b16 v[192:193], v6 offset:4096\n"
    "v_mfma_f32_16x16x32_bf16 v[72:75], v[176:179], a[212:215], v[72:75]\n"
    "ds_read_b64_tr_b16 v[194:195], v6 offset:4608\n"
    "v_mfma_f32_16x16x32_bf16 v[76:79], v[180:183], a[212:215], v[76:79]\n"
    "s_waitcnt lgkmcnt(9)\n"
    "buffer_load_dwordx4 v1, s[44:47], s75 offen offset:1024 lds\n"
    "v_mfma_f32_16x16x32_bf16 v[80:83], v[168:171], a[216:219], v[80:83]\n"
    "ds_read_b64_tr_b16 v[196:197], v7 offset:4096\n"
    "ds_read_b64_tr_b16 v[198:199], v7 offset:4608\n"
    "v_mfma_f32_16x16x32_bf16 v[84:87], v[172:175], a[216:219], v[84:87]\n"
    "v_mfma_f32_16x16x32_bf16 v[88:91], v[176:179], a[216:219], v[88:91]\n"
    "ds_read_b128 a[232:235], v14 offset:12288\n"
    "v_mfma_f32_16x16x32_bf16 v[92:95], v[180:183], a[216:219], v[92:95]\n"
    "s_waitcnt lgkmcnt(11)\n"
    "ds_read_b128 a[236:239], v14 offset:14336\n"
    "v_mfma_f32_16x16x32_bf16 v[96:99], v[168:171], a[220:223], v[96:99]\n"
    "ds_read_b128 a[240:243], v14 offset:16384\n"
    "v_mfma_f32_16x16x32_bf16 v[100:103], v[172:175], a[220:223], v[100:103]\n"
    "ds_read_b128 a[244:247], v14 offset:18432\n"
    "v_mfma_f32_16x16x32_bf16 v[104:107], v[176:179], a[220:223], v[104:107]\n"
    "buffer_load_dwordx4 v2, s[44:47], s75 offen offset:2048 lds\n"
    "v_mfma_f32_16x16x32_bf16 v[108:111], v[180:183], a[220:223], v[108:111]\n"
    "s_waitcnt lgkmcnt(13)\n"
    "ds_read_b128 a[248:251], v14 offset:20480\n"
    "ds_read_b128 a[252:255], v14 offset:22528\n"
    "v_mfma_f32_16x16x32_bf16 v[112:115], v[168:171], a[224:227], v[112:115]\n"
    "v_mfma_f32_16x16x32_bf16 v[116:119], v[172:175], a[224:227], v[116:119]\n"
    "buffer_load_dwordx4 v3, s[44:47], s75 offen offset:3072 lds\n"
    "v_mfma_f32_16x16x32_bf16 v[120:123], v[176:179], a[224:227], v[120:123]\n"
    "v_mfma_f32_16x16x32_bf16 v[124:127], v[180:183], a[224:227], v[124:127]\n"
    "s_waitcnt lgkmcnt(14)\n"
    "v_mfma_f32_16x16x32_bf16 v[128:131], v[168:171], a[228:231], v[128:131]\n"
    "v_mfma_f32_16x16x32_bf16 v[132:135], v[172:175], a[228:231], v[132:135]\n"
    "v_mfma_f32_16x16x32_bf16 v[136:139], v[176:179], a[228:231], v[136:139]\n"
    "v_mfma_f32_16x16x32_bf16 v[140:143], v[180:183], a[228:231], v[140:143]\n"
    "s_waitcnt lgkmcnt(5)\n"
    "v_mfma_f32_16x16x32_bf16 v[48:51], v[184:187], a[232:235], v[48:51]\n"
    "v_mfma_f32_16x16x32_bf16 v[52:55], v[188:191], a[232:235], v[52:55]\n"
    "s_waitcnt vmcnt(14)\n"
    "ds_read_b64_tr_b16 v[168:169], v4 offset:8192\n"
    "v_mfma_f32_16x16x32_bf16 v[56:59], v[192:195], a[232:235], v[56:59]\n"
    "s_waitcnt lgkmcnt(1)\n"
    "s_barrier\n"
    "ds_read_b64_tr_b16 v[170:171], v4 offset:8704\n"
    "v_mfma_f32_16x16x32_bf16 v[60:63], v[196:199], a[232:235], v[60:63]\n"
    "s_add_u32 m0, s65, 4096\n"
    "s_mov_b32 s75, 2359296\n"
    "ds_read_b64_tr_b16 v[172:173], v5 offset:8192\n"
    "v_mfma_f32_16x16x32_bf16 v[64:67], v[184:187], a[236:239], v[64:67]\n"
    "buffer_load_dwordx4 v0, s[44:47], s75 offen offset:0 lds\n"
    "v_mfma_f32_16x16x32_bf16 v[68:71], v[188:191], a[236:239], v[68:71]\n"
    "ds_read_b64_tr_b16 v[174:175], v5 offset:8704\n"
    "ds_read_b64_tr_b16 v[176:177], v6 offset:8192\n"
    "v_mfma_f32_16x16x32_bf16 v[72:75], v[192:195], a[236:239], v[72:75]\n"
    "buffer_load_dwordx4 v1, s[44:47], s75 offen offset:1024 lds\n"
    "v_mfma_f32_16x16x32_bf16 v[76:79], v[196:199], a[236:239], v[76:79]\n"
    "ds_read_b64_tr_b16 v[178:179], v6 offset:8704\n"
    "ds_read_b64_tr_b16 v[180:181], v7 offset:8192\n"
    "v_mfma_f32_16x16x32_bf16 v[80:83], v[184:187], a[240:243], v[80:83]\n"
    "ds_read_b64_tr_b16 v[182:183], v7 offset:8704\n"
    "v_mfma_f32_16x16x32_bf16 v[84:87], v[188:191], a[240:243], v[84:87]\n"
    "buffer_load_dwordx4 v2, s[44:47], s75 offen offset:2048 lds\n"
    "v_mfma_f32_16x16x32_bf16 v[88:91], v[192:195], a[240:243], v[88:91]\n"
    "ds_read_b128 a[208:211], v13 offset:0\n"
    "v_mfma_f32_16x16x32_bf16 v[92:95], v[196:199], a[240:243], v[92:95]\n"
    "ds_read_b128 a[212:215], v13 offset:2048\n"
    "v_mfma_f32_16x16x32_bf16 v[96:99], v[184:187], a[244:247], v[96:99]\n"
    "ds_read_b128 a[216:219], v13 offset:4096\n"
    "v_mfma_f32_16x16x32_bf16 v[100:103], v[188:191], a[244:247], v[100:103]\n"
    "buffer_load_dwordx4 v3, s[44:47], s75 offen offset:3072 lds\n"
    "v_mfma_f32_16x16x32_bf16 v[104:107], v[192:195], a[244:247], v[104:107]\n"
    "ds_read_b128 a[220:223], v13 offset:6144\n"
    "ds_read_b128 a[224:227], v13 offset:8192\n"
    "v_mfma_f32_16x16x32_bf16 v[108:111], v[196:199], a[244:247], v[108:111]\n"
    "s_add_u32 m0, s71, 143360\n"
    "s_mov_b32 s74, 640\n"
    "ds_read_b128 a[228:231], v13 offset:10240\n"
    "v_mfma_f32_16x16x32_bf16 v[112:115], v[184:187], a[248:251], v[112:115]\n"
    "buffer_load_dwordx4 v8, s[40:43], s74 offen offset:0 lds\n"
    "s_add_u32 m0, s71, 147456\n"
    "s_mov_b32 s74, 29312\n"
    "v_mfma_f32_16x16x32_bf16 v[116:119], v[188:191], a[248:251], v[116:119]\n"
    "buffer_load_dwordx4 v8, s[40:43], s74 offen offset:0 lds\n"
    "v_mfma_f32_16x16x32_bf16 v[120:123], v[192:195], a[248:251], v[120:123]\n"
    "v_mfma_f32_16x16x32_bf16 v[124:127], v[196:199], a[248:251], v[124:127]\n"
    "s_add_u32 m0, s71, 151552\n"
    "s_mov_b32 s74, 57984\n"
    "v_mfma_f32_16x16x32_bf16 v[128:131], v[184:187], a[252:255], v[128:131]\n"
    "buffer_load_dwordx4 v8, s[40:43], s74 offen offset:0 lds\n"
    "v_mfma_f32_16x16x32_bf16 v[132:135], v[188:191], a[252:255], v[132:135]\n"
    "v_mfma_f32_16x16x32_bf16 v[136:139], v[192:195], a[252:255], v[136:139]\n"
    "v_mfma_f32_16x16x32_bf16 v[140:143], v[196:199], a[252:255], v[140:143]\n"
    "s_waitcnt lgkmcnt(5)\n"
    "v_mfma_f32_16x16x32_bf16 v[48:51], v[168:171], a[208:211], v[48:51]\n"
    "v_mfma_f32_16x16x32_bf16 v[52:55], v[172:175], a[208:211], v[52:55]\n"
    "s_add_u32 m0, s65, 8192\n"
    "s_mov_b32 s75, 2621440\n"
    "s_waitcnt vmcnt(14)\n"
    "ds_read_b64_tr_b16 v[184:185], v4 offset:12288\n"
    "v_mfma_f32_16x16x32_bf16 v[56:59], v[176:179], a[208:211], v[56:59]\n"
    "buffer_load_dwordx4 v0, s[44:47], s75 offen offset:0 lds\n"
    "v_mfma_f32_16x16x32_bf16 v[60:63], v[180:183], a[208:211], v[60:63]\n"
    "s_waitcnt lgkmcnt(5)\n"
    "ds_read_b64_tr_b16 v[186:187], v4 offset:12800\n"
    "ds_read_b64_tr_b16 v[188:189], v5 offset:12288\n"
    "v_mfma_f32_16x16x32_bf16 v[64:67], v[168:171], a[212:215], v[64:67]\n"
    "buffer_load_dwordx4 v1, s[44:47], s75 offen offset:1024 lds\n"
    "v_mfma_f32_16x16x32_bf16 v[68:71], v[172:175], a[212:215], v[68:71]\n"
    "ds_read_b64_tr_b16 v[190:191], v5 offset:12800\n"
    "ds_read_b64_tr_b16 v[192:193], v6 offset:12288\n"
    "v_mfma_f32_16x16x32_bf16 v[72:75], v[176:179], a[212:215], v[72:75]\n"
    "ds_read_b64_tr_b16 v[194:195], v6 offset:12800\n"
    "v_mfma_f32_16x16x32_bf16 v[76:79], v[180:183], a[212:215], v[76:79]\n"
    "s_waitcnt lgkmcnt(9)\n"
    "buffer_load_dwordx4 v2, s[44:47], s75 offen offset:2048 lds\n"
    "v_mfma_f32_16x16x32_bf16 v[80:83], v[168:171], a[216:219], v[80:83]\n"
    "ds_read_b64_tr_b16 v[196:197], v7 offset:12288\n"
    "ds_read_b64_tr_b16 v[198:199], v7 offset:12800\n"
    "v_mfma_f32_16x16x32_bf16 v[84:87], v[172:175], a[216:219], v[84:87]\n"
    "buffer_load_dwordx4 v3, s[44:47], s75 offen offset:3072 lds\n"
    "v_mfma_f32_16x16x32_bf16 v[88:91], v[176:179], a[216:219], v[88:91]\n"
    "ds_read_b128 a[232:235], v14 offset:0\n"
    "v_mfma_f32_16x16x32_bf16 v[92:95], v[180:183], a[216:219], v[92:95]\n"
    "s_waitcnt lgkmcnt(11)\n"
    "ds_read_b128 a[236:239], v14 offset:2048\n"
    "v_mfma_f32_16x16x32_bf16 v[96:99], v[168:171], a[220:223], v[96:99]\n"
    "ds_read_b128 a[240:243], v14 offset:4096\n"
    "v_mfma_f32_16x16x32_bf16 v[100:103], v[172:175], a[220:223], v[100:103]\n"
    "s_add_u32 m0, s64, 0\n"
    "ds_read_b128 a[244:247], v14 offset:6144\n"
    "v_mfma_f32_16x16x32_bf16 v[104:107], v[176:179], a[220:223], v[104:107]\n"
    "buffer_load_dwordx4 v208, s[48:51], s66 offen offset:0 nt lds\n"
    "v_mfma_f32_16x16x32_bf16 v[108:111], v[180:183], a[220:223], v[108:111]\n"
    "s_waitcnt lgkmcnt(13)\n"
    "ds_read_b128 a[248:251], v14 offset:8192\n"
    "ds_read_b128 a[252:255], v14 offset:10240\n"
    "v_mfma_f32_16x16x32_bf16 v[112:115], v[168:171], a[224:227], v[112:115]\n"
    "buffer_load_dwordx4 v209, s[48:51], s66 offen offset:1024 nt lds\n"
    "v_mfma_f32_16x16x32_bf16 v[116:119], v[172:175], a[224:227], v[116:119]\n"
    "buffer_load_dwordx4 v210, s[48:51], s66 offen offset:2048 nt lds\n"
    "v_mfma_f32_16x16x32_bf16 v[120:123], v[176:179], a[224:227], v[120:123]\n"
    "v_mfma_f32_16x16x32_bf16 v[124:127], v[180:183], a[224:227], v[124:127]\n"
    "s_waitcnt lgkmcnt(14)\n"
    "v_mfma_f32_16x16x32_bf16 v[128:131], v[168:171], a[228:231], v[128:131]\n"
    "buffer_load_dwordx4 v211, s[48:51], s66 offen offset:3072 nt lds\n"
    "v_mfma_f32_16x16x32_bf16 v[132:135], v[172:175], a[228:231], v[132:135]\n"
    "v_mfma_f32_16x16x32_bf16 v[136:139], v[176:179], a[228:231], v[136:139]\n"
    "v_mfma_f32_16x16x32_bf16 v[140:143], v[180:183], a[228:231], v[140:143]\n"
    "s_waitcnt lgkmcnt(5)\n"
    "v_mfma_f32_16x16x32_bf16 v[48:51], v[184:187], a[232:235], v[48:51]\n"
    "v_mfma_f32_16x16x32_bf16 v[52:55], v[188:191], a[232:235], v[52:55]\n"
    "s_waitcnt vmcnt(15)\n"
    "ds_read_b64_tr_b16 v[168:169], v4 offset:0\n"
    "v_mfma_f32_16x16x32_bf16 v[56:59], v[192:195], a[232:235], v[56:59]\n"
    "s_waitcnt lgkmcnt(1)\n"
    "s_barrier\n"
    "ds_read_b64_tr_b16 v[170:171], v4 offset:512\n"
    "v_mfma_f32_16x16x32_bf16 v[60:63], v[196:199], a[232:235], v[60:63]\n"
    "s_add_u32 s66, s66, s68\n"
    "s_add_u32 m0, s65, 12288\n"
    "s_mov_b32 s75, 2883584\n"
    "ds_read_b64_tr_b16 v[172:173], v5 offset:0\n"
    "v_mfma_f32_16x16x32_bf16 v[64:67], v[184:187], a[236:239], v[64:67]\n"
    "buffer_load_dwordx4 v0, s[44:47], s75 offen offset:0 lds\n"
    "v_mfma_f32_16x16x32_bf16 v[68:71], v[188:191], a[236:239], v[68:71]\n"
    "ds_read_b64_tr_b16 v[174:175], v5 offset:512\n"
    "ds_read_b64_tr_b16 v[176:177], v6 offset:0\n"
    "v_mfma_f32_16x16x32_bf16 v[72:75], v[192:195], a[236:239], v[72:75]\n"
    "buffer_load_dwordx4 v1, s[44:47], s75 offen offset:1024 lds\n"
    "v_mfma_f32_16x16x32_bf16 v[76:79], v[196:199], a[236:239], v[76:79]\n"
    "ds_read_b64_tr_b16 v[178:179], v6 offset:512\n"
    "ds_read_b64_tr_b16 v[180:181], v7 offset:0\n"
    "v_mfma_f32_16x16x32_bf16 v[80:83], v[184:187], a[240:243], v[80:83]\n"
    "ds_read_b64_tr_b16 v[182:183], v7 offset:512\n"
    "v_mfma_f32_16x16x32_bf16 v[84:87], v[188:191], a[240:243], v[84:87]\n"
    "buffer_load_dwordx4 v2, s[44:47], s75 offen offset:2048 lds\n"
    "v_mfma_f32_16x16x32_bf16 v[88:91], v[192:195], a[240:243], v[88:91]\n"
    "ds_read_b128 a[208:211], v13 offset:24576\n"
    "v_mfma_f32_16x16x32_bf16 v[92:95], v[196:199], a[240:243], v[92:95]\n"
    "ds_read_b128 a[212:215], v13 offset:26624\n"
    "v_mfma_f32_16x16x32_bf16 v[96:99], v[184:187], a[244:247], v[96:99]\n"
    "ds_read_b128 a[216:219], v13 offset:28672\n"
    "v_mfma_f32_16x16x32_bf16 v[100:103], v[188:191], a[244:247], v[100:103]\n"
    "buffer_load_dwordx4 v3, s[44:47], s75 offen offset:3072 lds\n"
    "v_mfma_f32_16x16x32_bf16 v[104:107], v[192:195], a[244:247], v[104:107]\n"
    "ds_read_b128 a[220:223], v13 offset:30720\n"
    "ds_read_b128 a[224:227], v11 offset:61440\n"
    "v_mfma_f32_16x16x32_bf16 v[108:111], v[196:199], a[244:247], v[108:111]\n"
    "s_add_u32 m0, s71, 131072\n"
    "s_mov_b32 s74, 768\n"
    "ds_read_b128 a[228:231], v11 offset:63488\n"
    "v_mfma_f32_16x16x32_bf16 v[112:115], v[184:187], a[248:251], v[112:115]\n"
    "buffer_load_dwordx4 v8, s[40:43], s74 offen offset:0 lds\n"
    "s_add_u32 m0, s71, 135168\n"
    "s_mov_b32 s74, 29440\n"
    "v_mfma_f32_16x16x32_bf16 v[116:119], v[188:191], a[248:251], v[116:119]\n"
    "buffer_load_dwordx4 v8, s[40:43], s74 offen offset:0 lds\n"
    "v_mfma_f32_16x16x32_bf16 v[120:123], v[192:195], a[248:251], v[120:123]\n"
    "v_mfma_f32_16x16x32_bf16 v[124:127], v[196:199], a[248:251], v[124:127]\n"
    "s_add_u32 m0, s71, 139264\n"
    "s_mov_b32 s74, 58112\n"
    "v_mfma_f32_16x16x32_bf16 v[128:131], v[184:187], a[252:255], v[128:131]\n"
    "buffer_load_dwordx4 v8, s[40:43], s74 offen offset:0 lds\n"
    "v_mfma_f32_16x16x32_bf16 v[132:135], v[188:191], a[252:255], v[132:135]\n"
    "v_mfma_f32_16x16x32_bf16 v[136:139], v[192:195], a[252:255], v[136:139]\n"
    "v_mfma_f32_16x16x32_bf16 v[140:143], v[196:199], a[252:255], v[140:143]\n"
    "s_waitcnt lgkmcnt(5)\n"
    "v_mfma_f32_16x16x32_bf16 v[48:51], v[168:171], a[208:211], v[48:51]\n"
    "v_mfma_f32_16x16x32_bf16 v[52:55], v[172:175], a[208:211], v[52:55]\n"
    "s_add_u32 m0, s65, 0\n"
    "s_mov_b32 s75, 3145728\n"
    "s_waitcnt vmcnt(18)\n"
    "ds_read_b64_tr_b16 v[184:185], v4 offset:4096\n"
    "v_mfma_f32_16x16x32_bf16 v[56:59], v[176:179], a[208:211], v[56:59]\n"
    "buffer_load_dwordx4 v0, s[44:47], s75 offen offset:0 lds\n"
    "v_mfma_f32_16x16x32_bf16 v[60:63], v[180:183], a[208:211], v[60:63]\n"
    "s_waitcnt lgkmcnt(5)\n"
    "ds_read_b64_tr_b16 v[186:187], v4 offset:4608\n"
    "ds_read_b64_tr_b16 v[188:189], v5 offset:4096\n"
    "v_mfma_f32_16x16x32_bf16 v[64:67], v[168:171], a[212:215], v[64:67]\n"
    "buffer_load_dwordx4 v1, s[44:47], s75 offen offset:1024 lds\n"
    "v_mfma_f32_16x16x32_bf16 v[68:71], v[172:175], a[212:215], v[68:71]\n"
    "ds_read_b64_tr_b16 v[190:191], v5 offset:4608\n"
    "ds_read_b64_tr_b16 v[192:193], v6 offset:4096\n"
    "v_mfma_f32_16x16x32_bf16 v[72:75], v[176:179], a[212:215], v[72:75]\n"
    "ds_read_b64_tr_b16 v[194:195], v6 offset:4608\n"
    "v_mfma_f32_16x16x32_bf16 v[76:79], v[180:183], a[212:215], v[76:79]\n"
    "s_waitcnt lgkmcnt(9)\n"
    "buffer_load_dwordx4 v2, s[44:47], s75 offen offset:2048 lds\n"
    "v_mfma_f32_16x16x32_bf16 v[80:83], v[168:171], a[216:219], v[80:83]\n"
    "ds_read_b64_tr_b16 v[196:197], v7 offset:4096\n"
    "ds_read_b64_tr_b16 v[198:199], v7 offset:4608\n"
    "v_mfma_f32_16x16x32_bf16 v[84:87], v[172:175], a[216:219], v[84:87]\n"
    "buffer_load_dwordx4 v3, s[44:47], s75 offen offset:3072 lds\n"
    "v_mfma_f32_16x16x32_bf16 v[88:91], v[176:179], a[216:219], v[88:91]\n"
    "ds_read_b128 a[232:235], v14 offset:24576\n"
    "v_mfma_f32_16x16x32_bf16 v[92:95], v[180:183], a[216:219], v[92:95]\n"
    "s_waitcnt lgkmcnt(11)\n"
    "ds_read_b128 a[236:239], v14 offset:26624\n"
    "v_mfma_f32_16x16x32_bf16 v[96:99], v[168:171], a[220:223], v[96:99]\n"
    "ds_read_b128 a[240:243], v14 offset:28672\n"
    "v_mfma_f32_16x16x32_bf16 v[100:103], v[172:175], a[220:223], v[100:103]\n"
    "s_add_u32 m0, s64, 4096\n"
    "ds_read_b128 a[244:247], v14 offset:30720\n"
    "v_mfma_f32_16x16x32_bf16 v[104:107], v[176:179], a[220:223], v[104:107]\n"
    "buffer_load_dwordx4 v208, s[48:51], s66 offen offset:0 nt lds\n"
    "v_mfma_f32_16x16x32_bf16 v[108:111], v[180:183], a[220:223], v[108:111]\n"
    "s_waitcnt lgkmcnt(13)\n"
    "ds_read_b128 a[248:251], v12 offset:61440\n"
    "ds_read_b128 a[252:255], v12 offset:63488\n"
    "v_mfma_f32_16x16x32_bf16 v[112:115], v[168:171], a[224:227], v[112:115]\n"
    "buffer_load_dwordx4 v209, s[48:51], s66 offen offset:1024 nt lds\n"
    "v_mfma_f32_16x16x32_bf16 v[116:119], v[172:175], a[224:227], v[116:119]\n"
    "buffer_load_dwordx4 v210, s[48:51], s66 offen offset:2048 nt lds\n"
    "v_mfma_f32_16x16x32_bf16 v[120:123], v[176:179], a[224:227], v[120:123]\n"
    "v_mfma_f32_16x16x32_bf16 v[124:127], v[180:183], a[224:227], v[124:127]\n"
    "s_waitcnt lgkmcnt(14)\n"
    "v_mfma_f32_16x16x32_bf16 v[128:131], v[168:171], a[228:231], v[128:131]\n"
    "buffer_load_dwordx4 v211, s[48:51], s66 offen offset:3072 nt lds\n"
    "v_mfma_f32_16x16x32_bf16 v[132:135], v[172:175], a[228:231], v[132:135]\n"
    "v_mfma_f32_16x16x32_bf16 v[136:139], v[176:179], a[228:231], v[136:139]\n"
    "v_mfma_f32_16x16x32_bf16 v[140:143], v[180:183], a[228:231], v[140:143]\n"
    "s_waitcnt lgkmcnt(5)\n"
    "v_mfma_f32_16x16x32_bf16 v[48:51], v[184:187], a[232:235], v[48:51]\n"
    "v_mfma_f32_16x16x32_bf16 v[52:55], v[188:191], a[232:235], v[52:55]\n"
    "s_waitcnt vmcnt(19)\n"
    "ds_read_b64_tr_b16 v[168:169], v4 offset:8192\n"
    "v_mfma_f32_16x16x32_bf16 v[56:59], v[192:195], a[232:235], v[56:59]\n"
    "s_waitcnt lgkmcnt(1)\n"
    "s_barrier\n"
    "ds_read_b64_tr_b16 v[170:171], v4 offset:8704\n"
    "v_mfma_f32_16x16x32_bf16 v[60:63], v[196:199], a[232:235], v[60:63]\n"
    "s_add_u32 s66, s66, s68\n"
    "s_add_u32 m0, s65, 4096\n"
    "s_mov_b32 s75, 3407872\n"
    "ds_read_b64_tr_b16 v[172:173], v5 offset:8192\n"
    "v_mfma_f32_16x16x32_bf16 v[64:67], v[184:187], a[236:239], v[64:67]\n"
    "buffer_load_dwordx4 v0, s[44:47], s75 offen offset:0 lds\n"
    "v_mfma_f32_16x16x32_bf16 v[68:71], v[188:191], a[236:239], v[68:71]\n"
    "ds_read_b64_tr_b16 v[174:175], v5 offset:8704\n"
    "ds_read_b64_tr_b16 v[176:177], v6 offset:8192\n"
    "v_mfma_f32_16x16x32_bf16 v[72:75], v[192:195], a[236:239], v[72:75]\n"
    "buffer_load_dwordx4 v1, s[44:47], s75 offen offset:1024 lds\n"
    "v_mfma_f32_16x16x32_bf16 v[76:79], v[196:199], a[236:239], v[76:79]\n"
    "ds_read_b64_tr_b16 v[178:179], v6 offset:8704\n"
    "ds_read_b64_tr_b16 v[180:181], v7 offset:8192\n"
    "v_mfma_f32_16x16x32_bf16 v[80:83], v[184:187], a[240:243], v[80:83]\n"
    "ds_read_b64_tr_b16 v[182:183], v7 offset:8704\n"
    "v_mfma_f32_16x16x32_bf16 v[84:87], v[188:191], a[240:243], v[84:87]\n"
    "buffer_load_dwordx4 v2, s[44:47], s75 offen offset:2048 lds\n"
    "v_mfma_f32_16x16x32_bf16 v[88:91], v[192:195], a[240:243], v[88:91]\n"
    "ds_read_b128 a[208:211], v13 offset:12288\n"
    "v_mfma_f32_16x16x32_bf16 v[92:95], v[196:199], a[240:243], v[92:95]\n"
    "ds_read_b128 a[212:215], v13 offset:14336\n"
    "v_mfma_f32_16x16x32_bf16 v[96:99], v[184:187], a[244:247], v[96:99]\n"
    "buffer_load_dwordx4 v3, s[44:47], s75 offen offset:3072 lds\n"
    "v_mfma_f32_16x16x32_bf16 v[100:103], v[188:191], a[244:247], v[100:103]\n"
    "ds_read_b128 a[216:219], v13 offset:16384\n"
    "ds_read_b128 a[220:223], v13 offset:18432\n"
    "v_mfma_f32_16x16x32_bf16 v[104:107], v[192:195], a[244:247], v[104:107]\n"
    "s_add_u32 m0, s64, 8192\n"
    "ds_read_b128 a[224:227], v13 offset:20480\n"
    "v_mfma_f32_16x16x32_bf16 v[108:111], v[196:199], a[244:247], v[108:111]\n"
    "buffer_load_dwordx4 v208, s[48:51], s66 offen offset:0 nt lds\n"
    "v_mfma_f32_16x16x32_bf16 v[112:115], v[184:187], a[248:251], v[112:115]\n"
    "ds_read_b128 a[228:231], v13 offset:22528\n"
    "v_mfma_f32_16x16x32_bf16 v[116:119], v[188:191], a[248:251], v[116:119]\n"
    "buffer_load_dwordx4 v209, s[48:51], s66 offen offset:1024 nt lds\n"
    "v_mfma_f32_16x16x32_bf16 v[120:123], v[192:195], a[248:251], v[120:123]\n"
    "buffer_load_dwordx4 v210, s[48:51], s66 offen offset:2048 nt lds\n"
    "v_mfma_f32_16x16x32_bf16 v[124:127], v[196:199], a[248:251], v[124:127]\n"
    "v_mfma_f32_16x16x32_bf16 v[128:131], v[184:187], a[252:255], v[128:131]\n"
    "buffer_load_dwordx4 v211, s[48:51], s66 offen offset:3072 nt lds\n"
    "v_mfma_f32_16x16x32_bf16 v[132:135], v[188:191], a[252:255], v[132:135]\n"
    "v_mfma_f32_16x16x32_bf16 v[136:139], v[192:195], a[252:255], v[136:139]\n"
    "v_mfma_f32_16x16x32_bf16 v[140:143], v[196:199], a[252:255], v[140:143]\n"
    "s_waitcnt lgkmcnt(5)\n"
    "v_mfma_f32_16x16x32_bf16 v[48:51], v[168:171], a[208:211], v[48:51]\n"
    "v_mfma_f32_16x16x32_bf16 v[52:55], v[172:175], a[208:211], v[52:55]\n"
    "s_add_u32 s66, s66, s68\n"
    "s_add_u32 m0, s65, 8192\n"
    "s_add_u32 s75, s67, 32768\n"
    "s_waitcnt vmcnt(19)\n"
    "ds_read_b64_tr_b16 v[184:185], v4 offset:12288\n"
    "v_mfma_f32_16x16x32_bf16 v[56:59], v[176:179], a[208:211], v[56:59]\n"
    "buffer_load_dwordx4 v212, s[52:55], s75 offen offset:0 nt lds\n"
    "v_mfma_f32_16x16x32_bf16 v[60:63], v[180:183], a[208:211], v[60:63]\n"
    "s_waitcnt lgkmcnt(5)\n"
    "ds_read_b64_tr_b16 v[186:187], v4 offset:12800\n"
    "ds_read_b64_tr_b16 v[188:189], v5 offset:12288\n"
    "v_mfma_f32_16x16x32_bf16 v[64:67], v[168:171], a[212:215], v[64:67]\n"
    "ds_read_b64_tr_b16 v[190:191], v5 offset:12800\n"
    "v_mfma_f32_16x16x32_bf16 v[68:71], v[172:175], a[212:215], v[68:71]\n"
    "ds_read_b64_tr_b16 v[192:193], v6 offset:12288\n"
    "v_mfma_f32_16x16x32_bf16 v[72:75], v[176:179], a[212:215], v[72:75]\n"
    "ds_read_b64_tr_b16 v[194:195], v6 offset:12800\n"
    "v_mfma_f32_16x16x32_bf16 v[76:79], v[180:183], a[212:215], v[76:79]\n"
    "s_waitcnt lgkmcnt(9)\n"
    "buffer_load_dwordx4 v213, s[52:55], s75 offen offset:1024 nt lds\n"
    "v_mfma_f32_16x16x32_bf16 v[80:83], v[168:171], a[216:219], v[80:83]\n"
    "ds_read_b64_tr_b16 v[196:197], v7 offset:12288\n"
    "ds_read_b64_tr_b16 v[198:199], v7 offset:12800\n"
    "v_mfma_f32_16x16x32_bf16 v[84:87], v[172:175], a[216:219], v[84:87]\n"
    "v_mfma_f32_16x16x32_bf16 v[88:91], v[176:179], a[216:219], v[88:91]\n"
    "ds_read_b128 a[232:235], v14 offset:12288\n"
    "v_mfma_f32_16x16x32_bf16 v[92:95], v[180:183], a[216:219], v[92:95]\n"
    "s_waitcnt lgkmcnt(11)\n"
    "ds_read_b128 a[236:239], v14 offset:14336\n"
    "v_mfma_f32_16x16x32_bf16 v[96:99], v[168:171], a[220:223], v[96:99]\n"
    "ds_read_b128 a[240:243], v14 offset:16384\n"
    "v_mfma_f32_16x16x32_bf16 v[100:103], v[172:175], a[220:223], v[100:103]\n"
    "ds_read_b128 a[244:247], v14 offset:18432\n"
    "v_mfma_f32_16x16x32_bf16 v[104:107], v[176:179], a[220:223], v[104:107]\n"
    "buffer_load_dwordx4 v214, s[52:55], s75 offen offset:2048 nt lds\n"
    "v_mfma_f32_16x16x32_bf16 v[108:111], v[180:183], a[220:223], v[108:111]\n"
    "s_waitcnt lgkmcnt(13)\n"
    "ds_read_b128 a[248:251], v14 offset:20480\n"
    "ds_read_b128 a[252:255], v14 offset:22528\n"
    "v_mfma_f32_16x16x32_bf16 v[112:115], v[168:171], a[224:227], v[112:115]\n"
    "v_mfma_f32_16x16x32_bf16 v[116:119], v[172:175], a[224:227], v[116:119]\n"
    "buffer_load_dwordx4 v215, s[52:55], s75 offen offset:3072 nt lds\n"
    "v_mfma_f32_16x16x32_bf16 v[120:123], v[176:179], a[224:227], v[120:123]\n"
    "v_mfma_f32_16x16x32_bf16 v[124:127], v[180:183], a[224:227], v[124:127]\n"
    "s_waitcnt lgkmcnt(14)\n"
    "v_mfma_f32_16x16x32_bf16 v[128:131], v[168:171], a[228:231], v[128:131]\n"
    "v_mfma_f32_16x16x32_bf16 v[132:135], v[172:175], a[228:231], v[132:135]\n"
    "v_mfma_f32_16x16x32_bf16 v[136:139], v[176:179], a[228:231], v[136:139]\n"
    "v_mfma_f32_16x16x32_bf16 v[140:143], v[180:183], a[228:231], v[140:143]\n"
    "s_waitcnt lgkmcnt(5)\n"
    "v_mfma_f32_16x16x32_bf16 v[48:51], v[184:187], a[232:235], v[48:51]\n"
    "s_waitcnt vmcnt(16)\n"
    "ds_read_b64_tr_b16 v[168:169], v4 offset:0\n"
    "ds_read_b64_tr_b16 v[170:171], v4 offset:512\n"
    "v_mfma_f32_16x16x32_bf16 v[52:55], v[188:191], a[232:235], v[52:55]\n"
    "ds_read_b64_tr_b16 v[172:173], v5 offset:0\n"
    "v_mfma_f32_16x16x32_bf16 v[56:59], v[192:195], a[232:235], v[56:59]\n"
    "s_waitcnt lgkmcnt(3)\n"
    "s_barrier\n"
    "s_add_u32 m0, s64, 12288\n"
    "ds_read_b64_tr_b16 v[174:175], v5 offset:512\n"
    "ds_read_b64_tr_b16 v[176:177], v6 offset:0\n"
    "v_mfma_f32_16x16x32_bf16 v[60:63], v[196:199], a[232:235], v[60:63]\n"
    "buffer_load_dwordx4 v208, s[48:51], s66 offen offset:0 nt lds\n"
    "v_mfma_f32_16x16x32_bf16 v[64:67], v[184:187], a[236:239], v[64:67]\n"
    "ds_read_b64_tr_b16 v[178:179], v6 offset:512\n"
    "ds_read_b64_tr_b16 v[180:181], v7 offset:0\n"
    "v_mfma_f32_16x16x32_bf16 v[68:71], v[188:191], a[236:239], v[68:71]\n"
    "ds_read_b64_tr_b16 v[182:183], v7 offset:512\n"
    "s_waitcnt vmcnt(9)\n"
    "ds_read_b64_tr_b16 v[148:149], v4 offset:4096\n"
    "v_mfma_f32_16x16x32_bf16 v[72:75], v[192:195], a[236:239], v[72:75]\n"
    "ds_read_b64_tr_b16 v[150:151], v4 offset:4608\n"
    "v_mfma_f32_16x16x32_bf16 v[76:79], v[196:199], a[236:239], v[76:79]\n"
    "ds_read_b64_tr_b16 v[152:153], v5 offset:4096\n"
    "ds_read_b64_tr_b16 v[154:155], v5 offset:4608\n"
    "v_mfma_f32_16x16x32_bf16 v[80:83], v[184:187], a[240:243], v[80:83]\n"
    "buffer_load_dwordx4 v209, s[48:51], s66 offen offset:1024 nt lds\n"
    "v_mfma_f32_16x16x32_bf16 v[84:87], v[188:191], a[240:243], v[84:87]\n"
    "v_mfma_f32_16x16x32_bf16 v[88:91], v[192:195], a[240:243], v[88:91]\n"
    "v_mfma_f32_16x16x32_bf16 v[92:95], v[196:199], a[240:243], v[92:95]\n"
    "ds_read_b128 a[208:211], v13 offset:0\n"
    "v_mfma_f32_16x16x32_bf16 v[96:99], v[184:187], a[244:247], v[96:99]\n"
    "ds_read_b64_tr_b16 v[156:157], v6 offset:4096\n"
    "v_mfma_f32_16x16x32_bf16 v[100:103], v[188:191], a[244:247], v[100:103]\n"
    "ds_read_b64_tr_b16 v[158:159], v6 offset:4608\n"
    "ds_read_b64_tr_b16 v[160:161], v7 offset:4096\n"
    "v_mfma_f32_16x16x32_bf16 v[104:107], v[192:195], a[244:247], v[104:107]\n"
    "buffer_load_dwordx4 v210, s[48:51], s66 offen offset:2048 nt lds\n"
    "v_mfma_f32_16x16x32_bf16 v[108:111], v[196:199], a[244:247], v[108:111]\n"
    "ds_read_b64_tr_b16 v[162:163], v7 offset:4608\n"
    "ds_read_b128 a[0:3], v14 offset:0\n"
    "v_mfma_f32_16x16x32_bf16 v[112:115], v[184:187], a[248:251], v[112:115]\n"
    "ds_read_b128 a[212:215], v13 offset:2048\n"
    "ds_read_b128 a[4:7], v14 offset:2048\n"
    "v_mfma_f32_16x16x32_bf16 v[116:119], v[188:191], a[248:251], v[116:119]\n"
    "ds_read_b128 a[216:219], v13 offset:4096\n"
    "v_mfma_f32_16x16x32_bf16 v[120:123], v[192:195], a[248:251], v[120:123]\n"
    "buffer_load_dwordx4 v211, s[48:51], s66 offen offset:3072 nt lds\n"
    "v_mfma_f32_16x16x32_bf16 v[124:127], v[196:199], a[248:251], v[124:127]\n"
    "ds_read_b128 a[8:11], v14 offset:4096\n"
    "ds_read_b128 a[220:223], v13 offset:6144\n"
    "v_mfma_f32_16x16x32_bf16 v[128:131], v[184:187], a[252:255], v[128:131]\n"
    "ds_read_b128 a[12:15], v14 offset:6144\n"
    "ds_read_b128 a[224:227], v13 offset:8192\n"
    "v_mfma_f32_16x16x32_bf16 v[132:135], v[188:191], a[252:255], v[132:135]\n"
    "ds_read_b128 a[16:19], v14 offset:8192\n"
    "ds_read_b128 a[228:231], v13 offset:10240\n"
    "v_mfma_f32_16x16x32_bf16 v[136:139], v[192:195], a[252:255], v[136:139]\n"
    "ds_read_b128 a[20:23], v14 offset:10240\n"
    "v_mfma_f32_16x16x32_bf16 v[140:143], v[196:199], a[252:255], v[140:143]\n"
    "s_waitcnt lgkmcnt(15)\n"
    "v_mfma_f32_16x16x32_bf16 v[48:51], v[168:171], a[208:211], v[48:51]\n"
    "s_add_u32 s66, s66, s68\n"
    "s_add_u32 m0, s65, 0\n"
    "s_add_u32 s75, s67, 0\n"
    "v_mfma_f32_16x16x32_bf16 v[52:55], v[172:175], a[208:211], v[52:55]\n"
    "buffer_load_dwordx4 v212, s[52:55], s75 offen offset:0 nt lds\n"
    "v_mfma_f32_16x16x32_bf16 v[56:59], v[176:179], a[208:211], v[56:59]\n"
    "v_mfma_f32_16x16x32_bf16 v[60:63], v[180:183], a[208:211], v[60:63]\n"
    "s_waitcnt lgkmcnt(10)\n"
    "v_mfma_f32_16x16x32_bf16 v[48:51], v[148:151], a[0:3], v[48:51]\n"
    "v_mfma_f32_16x16x32_bf16 v[52:55], v[152:155], a[0:3], v[52:55]\n"
    "v_mfma_f32_16x16x32_bf16 v[56:59], v[156:159], a[0:3], v[56:59]\n"
    "buffer_load_dwordx4 v213, s[52:55], s75 offen offset:1024 nt lds\n"
    "v_mfma_f32_16x16x32_bf16 v[60:63], v[160:163], a[0:3], v[60:63]\n"
    "s_waitcnt lgkmcnt(9)\n"
    "v_mfma_f32_16x16x32_bf16 v[64:67], v[168:171], a[212:215], v[64:67]\n"
    "v_mfma_f32_16x16x32_bf16 v[68:71], v[172:175], a[212:215], v[68:71]\n"
    "s_waitcnt lgkmcnt(0)\n"
    "s_barrier\n"
    "v_mfma_f32_16x16x32_bf16 v[72:75], v[176:179], a[212:215], v[72:75]\n"
    "v_mul_f32 v48, s70, v48\n"
    "v_mul_f32 v49, s70, v49\n"
    "v_mfma_f32_16x16x32_bf16 v[76:79], v[180:183], a[212:215], v[76:79]\n"
    "v_mul_f32 v50, s70, v50\n"
    "v_mul_f32 v51, s70, v51\n"
    "v_mfma_f32_16x16x32_bf16 v[64:67], v[148:151], a[4:7], v[64:67]\n"
    "buffer_load_dwordx4 v214, s[52:55], s75 offen offset:2048 nt lds\n"
    "v_mfma_f32_16x16x32_bf16 v[68:71], v[152:155], a[4:7], v[68:71]\n"
    "v_cvt_pk_bf16_f32 v20, v48, v49\n"
    "v_cvt_pk_bf16_f32 v21, v50, v51\n"
    "v_mfma_f32_16x16x32_bf16 v[72:75], v[156:159], a[4:7], v[72:75]\n"
    "ds_write_b64 v9, v[20:21] offset:0\n"
    "v_mfma_f32_16x16x32_bf16 v[76:79], v[160:163], a[4:7], v[76:79]\n"
    "v_mul_f32 v52, s70, v52\n"
    "v_mul_f32 v53, s70, v53\n"
    "v_mfma_f32_16x16x32_bf16 v[80:83], v[168:171], a[216:219], v[80:83]\n"
    "v_mul_f32 v54, s70, v54\n"
    "v_mul_f32 v55, s70, v55\n"
    "v_mfma_f32_16x16x32_bf16 v[84:87], v[172:175], a[216:219], v[84:87]\n"
    "buffer_load_dwordx4 v215, s[52:55], s75 offen offset:3072 nt lds\n"
    "v_mfma_f32_16x16x32_bf16 v[88:91], v[176:179], a[216:219], v[88:91]\n"
    "v_cvt_pk_bf16_f32 v22, v52, v53\n"
    "v_cvt_pk_bf16_f32 v23, v54, v55\n"
    "v_mfma_f32_16x16x32_bf16 v[92:95], v[180:183], a[216:219], v[92:95]\n"
    "ds_write_b64 v9, v[22:23] offset:512\n"
    "v_mfma_f32_16x16x32_bf16 v[80:83], v[148:151], a[8:11], v[80:83]\n"
    "v_mul_f32 v56, s70, v56\n"
    "v_mul_f32 v57, s70, v57\n"
    "v_mfma_f32_16x16x32_bf16 v[84:87], v[152:155], a[8:11], v[84:87]\n"
    "v_mul_f32 v58, s70, v58\n"
    "v_mul_f32 v59, s70, v59\n"
    "v_mfma_f32_16x16x32_bf16 v[88:91], v[156:159], a[8:11], v[88:91]\n"
    "v_cvt_pk_bf16_f32 v24, v56, v57\n"
    "v_cvt_pk_bf16_f32 v25, v58, v59\n"
    "v_mfma_f32_16x16x32_bf16 v[92:95], v[160:163], a[8:11], v[92:95]\n"
    "ds_write_b64 v9, v[24:25] offset:6144\n"
    "v_mfma_f32_16x16x32_bf16 v[96:99], v[168:171], a[220:223], v[96:99]\n"
    "s_add_u32 m0, s65, 4096\n"
    "s_add_u32 s75, s67, 16384\n"
    "v_mul_f32 v60, s70, v60\n"
    "v_mul_f32 v61, s70, v61\n"
    "v_mfma_f32_16x16x32_bf16 v[100:103], v[172:175], a[220:223], v[100:103]\n"
    "buffer_load_dwordx4 v212, s[52:55], s75 offen offset:0 nt lds\n"
    "v_mfma_f32_16x16x32_bf16 v[104:107], v[176:179], a[220:223], v[104:107]\n"
    "v_mul_f32 v62, s70, v62\n"
    "v_mul_f32 v63, s70, v63\n"
    "v_mfma_f32_16x16x32_bf16 v[108:111], v[180:183], a[220:223], v[108:111]\n"
    "v_cvt_pk_bf16_f32 v26, v60, v61\n"
    "v_cvt_pk_bf16_f32 v27, v62, v63\n"
    "v_mfma_f32_16x16x32_bf16 v[96:99], v[148:151], a[12:15], v[96:99]\n"
    "ds_write_b64 v9, v[26:27] offset:6656\n"
    "v_mfma_f32_16x16x32_bf16 v[100:103], v[152:155], a[12:15], v[100:103]\n"
    "v_mul_f32 v64, s70, v64\n"
    "v_mul_f32 v65, s70, v65\n"
    "v_mfma_f32_16x16x32_bf16 v[104:107], v[156:159], a[12:15], v[104:107]\n"
    "buffer_load_dwordx4 v213, s[52:55], s75 offen offset:1024 nt lds\n"
    "v_mfma_f32_16x16x32_bf16 v[108:111], v[160:163], a[12:15], v[108:111]\n"
    "v_mul_f32 v66, s70, v66\n"
    "v_mul_f32 v67, s70, v67\n"
    "v_mfma_f32_16x16x32_bf16 v[112:115], v[168:171], a[224:227], v[112:115]\n"
    "v_cvt_pk_bf16_f32 v28, v64, v65\n"
    "v_cvt_pk_bf16_f32 v29, v66, v67\n"
    "v_mfma_f32_16x16x32_bf16 v[116:119], v[172:175], a[224:227], v[116:119]\n"
    "ds_write_b64 v9, v[28:29] offset:1024\n"
    "v_mfma_f32_16x16x32_bf16 v[120:123], v[176:179], a[224:227], v[120:123]\n"
    "v_mul_f32 v68, s70, v68\n"
    "v_mul_f32 v69, s70, v69\n"
    "v_mfma_f32_16x16x32_bf16 v[124:127], v[180:183], a[224:227], v[124:127]\n"
    "v_mul_f32 v70, s70, v70\n"
    "v_mul_f32 v71, s70, v71\n"
    "v_mfma_f32_16x16x32_bf16 v[112:115], v[148:151], a[16:19], v[112:115]\n"
    "buffer_load_dwordx4 v214, s[52:55], s75 offen offset:2048 nt lds\n"
    "v_mfma_f32_16x16x32_bf16 v[116:119], v[152:155], a[16:19], v[116:119]\n"
    "v_cvt_pk_bf16_f32 v30, v68, v69\n"
    "v_cvt_pk_bf16_f32 v31, v70, v71\n"
    "v_mfma_f32_16x16x32_bf16 v[120:123], v[156:159], a[16:19], v[120:123]\n"
    "ds_write_b64 v9, v[30:31] offset:1536\n"
    "v_mfma_f32_16x16x32_bf16 v[124:127], v[160:163], a[16:19], v[124:127]\n"
    "v_mul_f32 v72, s70, v72\n"
    "v_mul_f32 v73, s70, v73\n"
    "v_mfma_f32_16x16x32_bf16 v[128:131], v[168:171], a[228:231], v[128:131]\n"
    "v_mul_f32 v74, s70, v74\n"
    "v_mul_f32 v75, s70, v75\n"
    "v_mfma_f32_16x16x32_bf16 v[132:135], v[172:175], a[228:231], v[132:135]\n"
    "buffer_load_dwordx4 v215, s[52:55], s75 offen offset:3072 nt lds\n"
    "v_mfma_f32_16x16x32_bf16 v[136:139], v[176:179], a[228:231], v[136:139]\n"
    "v_cvt_pk_bf16_f32 v32, v72, v73\n"
    "v_cvt_pk_bf16_f32 v33, v74, v75\n"
    "v_mfma_f32_16x16x32_bf16 v[140:143], v[180:183], a[228:231], v[140:143]\n"
    "ds_write_b64 v9, v[32:33] offset:7168\n"
    "v_mfma_f32_16x16x32_bf16 v[128:131], v[148:151], a[20:23], v[128:131]\n"
    "v_mfma_f32_16x16x32_bf16 v[132:135], v[152:155], a[20:23], v[132:135]\n"
    "v_mfma_f32_16x16x32_bf16 v[136:139], v[156:159], a[20:23], v[136:139]\n"
    "v_mfma_f32_16x16x32_bf16 v[140:143], v[160:163], a[20:23], v[140:143]\n"
    "s_add_u32 s66, s66, s80\n"
    "s_and_b32 s66, s66, 0x7ffff\n"
    "v_pk_mul_f32 v[76:77], v[76:77], s[70:71] op_sel_hi:[1,0]\n"
    "v_pk_mul_f32 v[78:79], v[78:79], s[70:71] op_sel_hi:[1,0]\n"
    "v_cvt_pk_bf16_f32 v34, v76, v77\n"
    "v_cvt_pk_bf16_f32 v35, v78, v79\n"
    "ds_write_b64 v9, v[34:35] offset:7680\n"
    "v_pk_mul_f32 v[80:81], v[80:81], s[70:71] op_sel_hi:[1,0]\n"
    "v_pk_mul_f32 v[82:83], v[82:83], s[70:71] op_sel_hi:[1,0]\n"
    "v_cvt_pk_bf16_f32 v36, v80, v81\n"
    "v_cvt_pk_bf16_f32 v37, v82, v83\n"
    "ds_write_b64 v9, v[36:37] offset:2048\n"
    "v_pk_mul_f32 v[84:85], v[84:85], s[70:71] op_sel_hi:[1,0]\n"
    "v_pk_mul_f32 v[86:87], v[86:87], s[70:71] op_sel_hi:[1,0]\n"
    "v_cvt_pk_bf16_f32 v38, v84, v85\n"
    "v_cvt_pk_bf16_f32 v39, v86, v87\n"
    "ds_write_b64 v9, v[38:39] offset:2560\n"
    "v_pk_mul_f32 v[88:89], v[88:89], s[70:71] op_sel_hi:[1,0]\n"
    "v_pk_mul_f32 v[90:91], v[90:91], s[70:71] op_sel_hi:[1,0]\n"
    "v_cvt_pk_bf16_f32 v40, v88, v89\n"
    "v_cvt_pk_bf16_f32 v41, v90, v91\n"
    "ds_write_b64 v15, v[40:41] offset:12288\n"
    "v_pk_mul_f32 v[92:93], v[92:93], s[70:71] op_sel_hi:[1,0]\n"
    "v_pk_mul_f32 v[94:95], v[94:95], s[70:71] op_sel_hi:[1,0]\n"
    "v_cvt_pk_bf16_f32 v42, v92, v93\n"
    "v_cvt_pk_bf16_f32 v43, v94, v95\n"
    "ds_write_b64 v15, v[42:43] offset:12800\n"
    "v_pk_mul_f32 v[96:97], v[96:97], s[70:71] op_sel_hi:[1,0]\n"
    "v_pk_mul_f32 v[98:99], v[98:99], s[70:71] op_sel_hi:[1,0]\n"
    "v_cvt_pk_bf16_f32 v44, v96, v97\n"
    "v_cvt_pk_bf16_f32 v45, v98, v99\n"
    "ds_write_b64 v9, v[44:45] offset:3072\n"
    "v_pk_mul_f32 v[100:101], v[100:101], s[70:71] op_sel_hi:[1,0]\n"
    "v_pk_mul_f32 v[102:103], v[102:103], s[70:71] op_sel_hi:[1,0]\n"
    "v_cvt_pk_bf16_f32 v46, v100, v101\n"
    "v_cvt_pk_bf16_f32 v47, v102, v103\n"
    "ds_write_b64 v9, v[46:47] offset:3584\n"
    "v_pk_mul_f32 v[104:105], v[104:105], s[70:71] op_sel_hi:[1,0]\n"
    "v_pk_mul_f32 v[106:107], v[106:107], s[70:71] op_sel_hi:[1,0]\n"
    "v_cvt_pk_bf16_f32 v20, v104, v105\n"
    "v_cvt_pk_bf16_f32 v21, v106, v107\n"
    "ds_write_b64 v15, v[20:21] offset:13312\n"
    "v_pk_mul_f32 v[108:109], v[108:109], s[70:71] op_sel_hi:[1,0]\n"
    "v_pk_mul_f32 v[110:111], v[110:111], s[70:71] op_sel_hi:[1,0]\n"
    "v_cvt_pk_bf16_f32 v22, v108, v109\n"
    "v_cvt_pk_bf16_f32 v23, v110, v111\n"
    "ds_write_b64 v15, v[22:23] offset:13824\n"
    "v_pk_mul_f32 v[112:113], v[112:113], s[70:71] op_sel_hi:[1,0]\n"
    "v_pk_mul_f32 v[114:115], v[114:115], s[70:71] op_sel_hi:[1,0]\n"
    "v_cvt_pk_bf16_f32 v24, v112, v113\n"
    "v_cvt_pk_bf16_f32 v25, v114, v115\n"
    "ds_write_b64 v9, v[24:25] offset:4096\n"
    "v_pk_mul_f32 v[116:117], v[116:117], s[70:71] op_sel_hi:[1,0]\n"
    "v_pk_mul_f32 v[118:119], v[118:119], s[70:71] op_sel_hi:[1,0]\n"
    "v_cvt_pk_bf16_f32 v26, v116, v117\n"
    "v_cvt_pk_bf16_f32 v27, v118, v119\n"
    "ds_write_b64 v9, v[26:27] offset:4608\n"
    "v_pk_mul_f32 v[120:121], v[120:121], s[70:71] op_sel_hi:[1,0]\n"
    "v_pk_mul_f32 v[122:123], v[122:123], s[70:71] op_sel_hi:[1,0]\n"
    "v_cvt_pk_bf16_f32 v28, v120, v121\n"
    "v_cvt_pk_bf16_f32 v29, v122, v123\n"
    "ds_write_b64 v15, v[28:29] offset:14336\n"
    "v_pk_mul_f32 v[124:125], v[124:125], s[70:71] op_sel_hi:[1,0]\n"
    "v_pk_mul_f32 v[126:127], v[126:127], s[70:71] op_sel_hi:[1,0]\n"
    "v_cvt_pk_bf16_f32 v30, v124, v125\n"
    "v_cvt_pk_bf16_f32 v31, v126, v127\n"
    "ds_write_b64 v15, v[30:31] offset:14848\n"
    "v_pk_mul_f32 v[128:129], v[128:129], s[70:71] op_sel_hi:[1,0]\n"
    "v_pk_mul_f32 v[130:131], v[130:131], s[70:71] op_sel_hi:[1,0]\n"
    "v_cvt_pk_bf16_f32 v32, v128, v129\n"
    "v_cvt_pk_bf16_f32 v33, v130, v131\n"
    "ds_write_b64 v9, v[32:33] offset:5120\n"
    "v_pk_mul_f32 v[132:133], v[132:133], s[70:71] op_sel_hi:[1,0]\n"
    "v_pk_mul_f32 v[134:135], v[134:135], s[70:71] op_sel_hi:[1,0]\n"
    "v_cvt_pk_bf16_f32 v34, v132, v133\n"
    "v_cvt_pk_bf16_f32 v35, v134, v135\n"
    "ds_write_b64 v9, v[34:35] offset:5632\n"
    "v_pk_mul_f32 v[136:137], v[136:137], s[70:71] op_sel_hi:[1,0]\n"
    "v_pk_mul_f32 v[138:139], v[138:139], s[70:71] op_sel_hi:[1,0]\n"
    "v_cvt_pk_bf16_f32 v36, v136, v137\n"
    "v_cvt_pk_bf16_f32 v37, v138, v139\n"
    "ds_write_b64 v15, v[36:37] offset:15360\n"
    "v_pk_mul_f32 v[140:141], v[140:141], s[70:71] op_sel_hi:[1,0]\n"
    "v_pk_mul_f32 v[142:143], v[142:143], s[70:71] op_sel_hi:[1,0]\n"
    "v_cvt_pk_bf16_f32 v38, v140, v141\n"
    "v_cvt_pk_bf16_f32 v39, v142, v143\n"
    "ds_write_b64 v15, v[38:39] offset:15872\n"
    "ds_read_b128 a[192:195], v216 offset:0\n"
    "ds_read_b128 a[196:199], v216 offset:2048\n"
    "ds_read_b128 a[200:203], v217 offset:0\n"
    "ds_read_b128 a[204:207], v217 offset:2048\n"
    "v_mov_b32 v225, v17\n"
    "s_waitcnt lgkmcnt(4)\n"
    "s_barrier\n"
    "ds_read_b128 a[0:3], v223 offset:0\n"
    "ds_read_b128 a[32:35], v223 offset:1024\n"
    "ds_read_b128 a[4:7], v223 offset:6144\n"
    "ds_read_b128 a[36:39], v223 offset:7168\n"
    "ds_read_b128 a[8:11], v223 offset:8192\n"
    "ds_read_b128 a[40:43], v223 offset:9216\n"
    "s_waitcnt lgkmcnt(5)\n"
    "v_mfma_f32_16x16x32_bf16 v[0:3], a[192:195], a[0:3], 0\n"
    "s_waitcnt lgkmcnt(4)\n"
    "ds_read_b128 a[208:211], v216 offset:4096\n"
    "ds_read_b128 a[212:215], v216 offset:6144\n"
    "v_mfma_f32_16x16x32_bf16 v[8:11], a[192:195], a[32:35], 0\n"
    "ds_read_b128 a[216:219], v217 offset:4096\n"
    "ds_read_b128 a[220:223], v217 offset:6144\n"
    "v_mfma_f32_16x16x32_bf16 v[4:7], a[196:199], a[0:3], 0\n"
    "ds_read_b128 a[12:15], v223 offset:14336\n"
    "ds_read_b128 a[44:47], v223 offset:15360\n"
    "v_mfma_f32_16x16x32_bf16 v[12:15], a[196:199], a[32:35], 0\n"
    "s_waitcnt lgkmcnt(9)\n"
    "s_waitcnt vmcnt(16)\n"
    "ds_read_b128 a[224:227], v216 offset:8192\n"
    "ds_read_b128 a[228:231], v216 offset:10240\n"
    "v_mfma_f32_16x16x32_bf16 v[0:3], a[200:203], a[4:7], v[0:3]\n"
    "s_waitcnt lgkmcnt(10)\n"
    "ds_read_b128 a[16:19], v223 offset:16384\n"
    "ds_read_b128 a[48:51], v223 offset:17408\n"
    "v_mfma_f32_16x16x32_bf16 v[8:11], a[200:203], a[36:39], v[8:11]\n"
    "s_add_u32 m0, s64, 0\n"
    "ds_read_b128 a[232:235], v217 offset:8192\n"
    "ds_read_b128 a[236:239], v217 offset:10240\n"
    "v_mfma_f32_16x16x32_bf16 v[4:7], a[204:207], a[4:7], v[4:7]\n"
    "buffer_load_dwordx4 v208, s[48:51], s66 offen offset:0 nt lds\n"
    "v_mfma_f32_16x16x32_bf16 v[12:15], a[204:207], a[36:39], v[12:15]\n"
    "s_waitcnt lgkmcnt(11)\n"
    "ds_read_b128 a[20:23], v223 offset:22528\n"
    "ds_read_b128 a[52:55], v223 offset:23552\n"
    "v_mfma_f32_16x16x32_bf16 v[0:3], a[208:211], a[8:11], v[0:3]\n"
    "s_waitcnt vmcnt(9)\n"
    "ds_read_b128 a[240:243], v216 offset:12288\n"
    "ds_read_b128 a[244:247], v216 offset:14336\n"
    "v_mfma_f32_16x16x32_bf16 v[8:11], a[208:211], a[40:43], v[8:11]\n"
    "s_waitcnt lgkmcnt(14)\n"
    "ds_read_b128 a[24:27], v223 offset:24576\n"
    "ds_read_b128 a[56:59], v223 offset:25600\n"
    "v_mfma_f32_16x16x32_bf16 v[4:7], a[212:215], a[8:11], v[4:7]\n"
    "ds_read_b128 a[248:251], v217 offset:12288\n"
    "ds_read_b128 a[252:255], v217 offset:14336\n"
    "v_mfma_f32_16x16x32_bf16 v[12:15], a[212:215], a[40:43], v[12:15]\n"
    "s_waitcnt lgkmcnt(15)\n"
    "buffer_load_dwordx4 v209, s[48:51], s66 offen offset:1024 nt lds\n"
    "v_mfma_f32_16x16x32_bf16 v[0:3], a[216:219], a[12:15], v[0:3]\n"
    "s_waitcnt lgkmcnt(14)\n"
    "ds_read_b128 a[28:31], v223 offset:30720\n"
    "ds_read_b128 a[60:63], v223 offset:31744\n"
    "v_mfma_f32_16x16x32_bf16 v[8:11], a[216:219], a[44:47], v[8:11]\n"
    "v_mfma_f32_16x16x32_bf16 v[4:7], a[220:223], a[12:15], v[4:7]\n"
    "ds_read_b128 a[64:67], v223 offset:2048\n"
    "ds_read_b128 a[96:99], v223 offset:3072\n"
    "v_mfma_f32_16x16x32_bf16 v[12:15], a[220:223], a[44:47], v[12:15]\n"
    "s_waitcnt lgkmcnt(15)\n"
    "v_mfma_f32_16x16x32_bf16 v[0:3], a[224:227], a[16:19], v[0:3]\n"
    "s_waitcnt lgkmcnt(14)\n"
    "buffer_load_dwordx4 v210, s[48:51], s66 offen offset:2048 nt lds\n"
    "v_mfma_f32_16x16x32_bf16 v[8:11], a[224:227], a[48:51], v[8:11]\n"
    "ds_read_b128 a[68:71], v224 offset:12288\n"
    "ds_read_b128 a[100:103], v224 offset:13312\n"
    "v_mfma_f32_16x16x32_bf16 v[4:7], a[228:231], a[16:19], v[4:7]\n"
    "ds_read_b128 a[72:75], v223 offset:10240\n"
    "ds_read_b128 a[104:107], v223 offset:11264\n"
    "v_mfma_f32_16x16x32_bf16 v[12:15], a[228:231], a[48:51], v[12:15]\n"
    "s_waitcnt lgkmcnt(15)\n"
    "v_mfma_f32_16x16x32_bf16 v[0:3], a[232:235], a[20:23], v[0:3]\n"
    "s_waitcnt lgkmcnt(14)\n"
    "ds_read_b128 a[76:79], v224 offset:28672\n"
    "ds_read_b128 a[108:111], v224 offset:29696\n"
    "v_mfma_f32_16x16x32_bf16 v[8:11], a[232:235], a[52:55], v[8:11]\n"
    "buffer_load_dwordx4 v211, s[48:51], s66 offen offset:3072 nt lds\n"
    "v_mfma_f32_16x16x32_bf16 v[4:7], a[236:239], a[20:23], v[4:7]\n"
    "ds_read_b128 a[80:83], v223 offset:18432\n"
    "v_add_u32 v207, 1, v207\n"
    "v_mfma_f32_16x16x32_bf16 v[12:15], a[236:239], a[52:55], v[12:15]\n"
    "s_waitcnt lgkmcnt(14)\n"
    "ds_read_b128 a[112:115], v223 offset:19456\n"
    "v_mfma_f32_16x16x32_bf16 v[0:3], a[240:243], a[24:27], v[0:3]\n"
    "s_waitcnt lgkmcnt(14)\n"
    "ds_read_b128 a[84:87], v224 offset:45056\n"
    "ds_read_b128 a[116:119], v224 offset:46080\n"
    "v_mfma_f32_16x16x32_bf16 v[8:11], a[240:243], a[56:59], v[8:11]\n"
    "s_add_u32 s66, s66, s68\n"
    "s_add_u32 m0, s64, 4096\n"
    "v_readlane_b32 s89, v207, s86\n"
    "v_mfma_f32_16x16x32_bf16 v[4:7], a[244:247], a[24:27], v[4:7]\n"
    "buffer_load_dwordx4 v208, s[48:51], s66 offen offset:0 nt lds\n"
    "v_mfma_f32_16x16x32_bf16 v[12:15], a[244:247], a[56:59], v[12:15]\n"
    "s_waitcnt lgkmcnt(13)\n"
    "ds_read_b128 a[88:91], v223 offset:26624\n"
    "ds_read_b128 a[120:123], v223 offset:27648\n"
    "v_mfma_f32_16x16x32_bf16 v[0:3], a[248:251], a[28:31], v[0:3]\n"
    "s_waitcnt lgkmcnt(14)\n"
    "ds_read_b128 a[92:95], v224 offset:61440\n"
    "v_mov_b32 v196, s89\n"
    "v_mfma_f32_16x16x32_bf16 v[8:11], a[248:251], a[60:63], v[8:11]\n"
    "ds_read_b128 a[124:127], v224 offset:62464\n"
    "v_mfma_f32_16x16x32_bf16 v[4:7], a[252:255], a[28:31], v[4:7]\n"
    "v_mov_b32 v200, 0\n"
    "v_mov_b32 v201, 0\n"
    "v_mfma_f32_16x16x32_bf16 v[12:15], a[252:255], a[60:63], v[12:15]\n"
    "s_waitcnt lgkmcnt(15)\n"
    "buffer_load_dwordx4 v209, s[48:51], s66 offen offset:1024 nt lds\n"
    "v_mfma_f32_16x16x32_bf16 v[16:19], a[192:195], a[64:67], 0\n"
    "s_waitcnt lgkmcnt(14)\n"
    "buffer_store_dword v196, v225, s[96:99], 0 offen sc1\n"
    "v_mov_b32 v202, 0\n"
    "v_mfma_f32_16x16x32_bf16 v[24:27], a[192:195], a[96:99], 0\n"
    "v_mov_b32 v203, 0\n"
    "v_mov_b32 v204, 0\n"
    "v_mfma_f32_16x16x32_bf16 v[20:23], a[196:199], a[64:67], 0\n"
    "v_mov_b32 v205, 0\n"
    "v_mfma_f32_16x16x32_bf16 v[28:31], a[196:199], a[96:99], 0\n"
    "s_waitcnt lgkmcnt(13)\n"
    "v_mfma_f32_16x16x32_bf16 v[16:19], a[200:203], a[68:71], v[16:19]\n"
    "s_waitcnt lgkmcnt(12)\n"
    "s_barrier\n"
    "buffer_load_dwordx4 v210, s[48:51], s66 offen offset:2048 nt lds\n"
    "v_mfma_f32_16x16x32_bf16 v[24:27], a[200:203], a[100:103], v[24:27]\n"
    "v_mfma_f32_16x16x32_bf16 v[20:23], a[204:207], a[68:71], v[20:23]\n"
    "v_mfma_f32_16x16x32_bf16 v[28:31], a[204:207], a[100:103], v[28:31]\n"
    "s_waitcnt lgkmcnt(11)\n"
    "v_max3_f32 v144, v0, v1, v2\n"
    "v_max3_f32 v145, v8, v9, v10\n"
    "v_mfma_f32_16x16x32_bf16 v[16:19], a[208:211], a[72:75], v[16:19]\n"
    "s_waitcnt lgkmcnt(10)\n"
    "v_max3_f32 v144, v144, v3, v4\n"
    "v_max3_f32 v145, v145, v11, v12\n"
    "v_mfma_f32_16x16x32_bf16 v[24:27], a[208:211], a[104:107], v[24:27]\n"
    "buffer_load_dwordx4 v211, s[48:51], s66 offen offset:3072 nt lds\n"
    "v_mfma_f32_16x16x32_bf16 v[20:23], a[212:215], a[72:75], v[20:23]\n"
    "v_max3_f32 v144, v144, v5, v6\n"
    "v_max3_f32 v145, v145, v13, v14\n"
    "v_mfma_f32_16x16x32_bf16 v[28:31], a[212:215], a[104:107], v[28:31]\n"
    "s_waitcnt lgkmcnt(9)\n"
    "v_max_f32 v144, v144, v7\n"
    "v_max_f32 v145, v145, v15\n"
    "v_mfma_f32_16x16x32_bf16 v[16:19], a[216:219], a[76:79], v[16:19]\n"
    "s_waitcnt lgkmcnt(8)\n"
    "v_mov_b32 v150, v144\n"
    "v_mov_b32 v151, v145\n"
    "s_nop 0\n"
    "v_mfma_f32_16x16x32_bf16 v[24:27], a[216:219], a[108:111], v[24:27]\n"
    "v_permlane32_swap_b32 v144, v150\n"
    "v_permlane32_swap_b32 v145, v151\n"
    "v_mfma_f32_16x16x32_bf16 v[20:23], a[220:223], a[76:79], v[20:23]\n"
    "v_max_f32 v144, v144, v150\n"
    "v_max_f32 v145, v145, v151\n"
    "v_mfma_f32_16x16x32_bf16 v[28:31], a[220:223], a[108:111], v[28:31]\n"
    "s_waitcnt lgkmcnt(7)\n"
    "s_add_u32 s66, s66, s68\n"
    "s_add_u32 m0, s64, 8192\n"
    "v_mov_b32 v150, v144\n"
    "v_mov_b32 v151, v145\n"
    "v_mfma_f32_16x16x32_bf16 v[16:19], a[224:227], a[80:83], v[16:19]\n"
    "s_waitcnt lgkmcnt(6)\n"
    "s_nop 0\n"
    "buffer_load_dwordx4 v208, s[48:51], s66 offen offset:0 nt lds\n"
    "v_mfma_f32_16x16x32_bf16 v[24:27], a[224:227], a[112:115], v[24:27]\n"
    "v_permlane16_swap_b32 v144, v150\n"
    "v_permlane16_swap_b32 v145, v151\n"
    "v_mfma_f32_16x16x32_bf16 v[20:23], a[228:231], a[80:83], v[20:23]\n"
    "ds_read_b128 a[128:131], v223 offset:4096\n"
    "v_max_f32 v144, v144, v150\n"
    "v_mfma_f32_16x16x32_bf16 v[28:31], a[228:231], a[112:115], v[28:31]\n"
    "s_waitcnt lgkmcnt(6)\n"
    "ds_read_b128 a[160:163], v223 offset:5120\n"
    "v_max_f32 v145, v145, v151\n"
    "v_mfma_f32_16x16x32_bf16 v[16:19], a[232:235], a[84:87], v[16:19]\n"
    "s_waitcnt lgkmcnt(6)\n"
    "ds_write_b32 v228, v144 offset:0\n"
    "ds_write_b32 v228, v145 offset:256\n"
    "v_mfma_f32_16x16x32_bf16 v[24:27], a[232:235], a[116:119], v[24:27]\n"
    "buffer_load_dwordx4 v209, s[48:51], s66 offen offset:1024 nt lds\n"
    "v_mfma_f32_16x16x32_bf16 v[20:23], a[236:239], a[84:87], v[20:23]\n"
    "ds_read_b128 a[132:135], v224 offset:14336\n"
    "ds_read_b128 a[164:167], v224 offset:15360\n"
    "v_mfma_f32_16x16x32_bf16 v[28:31], a[236:239], a[116:119], v[28:31]\n"
    "s_waitcnt lgkmcnt(9)\n"
    "ds_read_b128 a[136:139], v223 offset:12288\n"
    "ds_read_b128 a[168:171], v223 offset:13312\n"
    "v_mfma_f32_16x16x32_bf16 v[16:19], a[240:243], a[88:91], v[16:19]\n"
    "s_waitcnt lgkmcnt(10)\n"
    "ds_read_b128 a[140:143], v224 offset:30720\n"
    "ds_read_b128 a[172:175], v224 offset:31744\n"
    "v_mfma_f32_16x16x32_bf16 v[24:27], a[240:243], a[120:123], v[24:27]\n"
    "ds_read_b128 a[144:147], v223 offset:20480\n"
    "ds_read_b128 a[176:179], v223 offset:21504\n"
    "v_mfma_f32_16x16x32_bf16 v[20:23], a[244:247], a[88:91], v[20:23]\n"
    "buffer_load_dwordx4 v210, s[48:51], s66 offen offset:2048 nt lds\n"
    "v_mfma_f32_16x16x32_bf16 v[28:31], a[244:247], a[120:123], v[28:31]\n"
    "s_waitcnt lgkmcnt(13)\n"
    "ds_read_b128 a[148:151], v224 offset:47104\n"
    "ds_read_b128 a[180:183], v224 offset:48128\n"
    "v_mfma_f32_16x16x32_bf16 v[16:19], a[248:251], a[92:95], v[16:19]\n"
    "s_waitcnt lgkmcnt(14)\n"
    "s_waitcnt lgkmcnt(10)\n"
    "s_barrier\n"
    "ds_read_b128 a[152:155], v223 offset:28672\n"
    "ds_read_b128 a[184:187], v223 offset:29696\n"
    "v_mfma_f32_16x16x32_bf16 v[24:27], a[248:251], a[124:127], v[24:27]\n"
    "ds_read_b128 v[168:171], v229 offset:0\n"
    "ds_read_b128 v[172:175], v229 offset:256\n"
    "v_mfma_f32_16x16x32_bf16 v[20:23], a[252:255], a[92:95], v[20:23]\n"
    "ds_read_b128 a[156:159], v224 offset:63488\n"
    "ds_read_b128 a[188:191], v224 offset:64512\n"
    "v_mfma_f32_16x16x32_bf16 v[28:31], a[252:255], a[124:127], v[28:31]\n"
    "buffer_load_dwordx4 v211, s[48:51], s66 offen offset:3072 nt lds\n"
    "v_mfma_f32_16x16x32_bf16 v[32:35], a[192:195], a[128:131], 0\n"
    "v_mfma_f32_16x16x32_bf16 v[40:43], a[192:195], a[160:163], 0\n"
    "v_mfma_f32_16x16x32_bf16 v[36:39], a[196:199], a[128:131], 0\n"
    "v_mfma_f32_16x16x32_bf16 v[44:47], a[196:199], a[160:163], 0\n"
    "s_waitcnt lgkmcnt(15)\n"
    "v_mfma_f32_16x16x32_bf16 v[32:35], a[200:203], a[132:135], v[32:35]\n"
    "s_waitcnt lgkmcnt(14)\n"
    "v_mfma_f32_16x16x32_bf16 v[40:43], a[200:203], a[164:167], v[40:43]\n"
    "s_add_u32 s66, s66, s68\n"
    "s_add_u32 m0, s64, 12288\n"
    "s_waitcnt lgkmcnt(3)\n"
    "v_max3_f32 v144, v168, v169, v170\n"
    "s_waitcnt lgkmcnt(2)\n"
    "v_max3_f32 v145, v172, v173, v174\n"
    "v_mfma_f32_16x16x32_bf16 v[36:39], a[204:207], a[132:135], v[36:39]\n"
    "buffer_load_dwordx4 v208, s[48:51], s66 offen offset:0 nt lds\n"
    "v_mfma_f32_16x16x32_bf16 v[44:47], a[204:207], a[164:167], v[44:47]\n"
    "v_max_f32 v144, v144, v171\n"
    "v_max_f32 v145, v145, v175\n"
    "v_mfma_f32_16x16x32_bf16 v[32:35], a[208:211], a[136:139], v[32:35]\n"
    "v_max3_f32 v146, v16, v17, v18\n"
    "v_max3_f32 v147, v24, v25, v26\n"
    "v_mfma_f32_16x16x32_bf16 v[40:43], a[208:211], a[168:171], v[40:43]\n"
    "v_max3_f32 v146, v146, v19, v20\n"
    "v_max3_f32 v147, v147, v27, v28\n"
    "v_mfma_f32_16x16x32_bf16 v[36:39], a[212:215], a[136:139], v[36:39]\n"
    "v_max3_f32 v146, v146, v21, v22\n"
    "v_max3_f32 v147, v147, v29, v30\n"
    "v_mfma_f32_16x16x32_bf16 v[44:47], a[212:215], a[168:171], v[44:47]\n"
    "v_max_f32 v146, v146, v23\n"
    "v_max_f32 v147, v147, v31\n"
    "v_mfma_f32_16x16x32_bf16 v[32:35], a[216:219], a[140:143], v[32:35]\n"
    "buffer_load_dwordx4 v209, s[48:51], s66 offen offset:1024 nt lds\n"
    "v_mfma_f32_16x16x32_bf16 v[40:43], a[216:219], a[172:175], v[40:43]\n"
    "v_mov_b32 v152, v146\n"
    "v_mov_b32 v153, v147\n"
    "s_nop 0\n"
    "v_mfma_f32_16x16x32_bf16 v[36:39], a[220:223], a[140:143], v[36:39]\n"
    "v_permlane32_swap_b32 v146, v152\n"
    "v_permlane32_swap_b32 v147, v153\n"
    "v_mfma_f32_16x16x32_bf16 v[44:47], a[220:223], a[172:175], v[44:47]\n"
    "v_max_f32 v146, v146, v152\n"
    "v_max_f32 v147, v147, v153\n"
    "v_mfma_f32_16x16x32_bf16 v[32:35], a[224:227], a[144:147], v[32:35]\n"
    "v_mov_b32 v152, v146\n"
    "v_mov_b32 v153, v147\n"
    "s_nop 0\n"
    "v_mfma_f32_16x16x32_bf16 v[40:43], a[224:227], a[176:179], v[40:43]\n"
    "v_permlane16_swap_b32 v146, v152\n"
    "v_permlane16_swap_b32 v147, v153\n"
    "v_mfma_f32_16x16x32_bf16 v[36:39], a[228:231], a[144:147], v[36:39]\n"
    "buffer_load_dwordx4 v210, s[48:51], s66 offen offset:2048 nt lds\n"
    "v_mfma_f32_16x16x32_bf16 v[44:47], a[228:231], a[176:179], v[44:47]\n"
    "v_max_f32 v146, v146, v152\n"
    "v_max_f32 v147, v147, v153\n"
    "v_mfma_f32_16x16x32_bf16 v[32:35], a[232:235], a[148:151], v[32:35]\n"
    "ds_write_b32 v228, v146 offset:512\n"
    "ds_write_b32 v228, v147 offset:768\n"
    "v_mfma_f32_16x16x32_bf16 v[40:43], a[232:235], a[180:183], v[40:43]\n"
    "buffer_load_dwordx4 v211, s[48:51], s66 offen offset:3072 nt lds\n"
    "s_add_u32 s66, s66, s68\n"
    "s_add_u32 s66, s66, s80\n"
    "s_and_b32 s66, s66, 0x7ffff\n"
    "v_mfma_f32_16x16x32_bf16 v[36:39], a[236:239], a[148:151], v[36:39]\n"
    "v_sub_f32 v232, 0, v144\n"
    "v_sub_f32 v233, 0, v144\n"
    "v_mfma_f32_16x16x32_bf16 v[44:47], a[236:239], a[180:183], v[44:47]\n"
    "v_sub_f32 v234, 0, v144\n"
    "v_sub_f32 v235, 0, v144\n"
    "v_mfma_f32_16x16x32_bf16 v[32:35], a[240:243], a[152:155], v[32:35]\n"
    "v_sub_f32 v236, 0, v145\n"
    "v_sub_f32 v237, 0, v145\n"
    "v_mfma_f32_16x16x32_bf16 v[40:43], a[240:243], a[184:187], v[40:43]\n"
    "v_sub_f32 v238, 0, v145\n"
    "v_sub_f32 v239, 0, v145\n"
    "v_mfma_f32_16x16x32_bf16 v[36:39], a[244:247], a[152:155], v[36:39]\n"
    "v_add_f32 v0, v232, v0\n"
    "v_add_f32 v1, v232, v1\n"
    "v_mfma_f32_16x16x32_bf16 v[44:47], a[244:247], a[184:187], v[44:47]\n"
    "s_waitcnt lgkmcnt(3)\n"
    "v_add_f32 v2, v232, v2\n"
    "v_add_f32 v3, v232, v3\n"
    "v_mfma_f32_16x16x32_bf16 v[32:35], a[248:251], a[156:159], v[32:35]\n"
    "s_waitcnt lgkmcnt(2)\n"
    "v_add_f32 v4, v232, v4\n"
    "v_add_f32 v5, v232, v5\n"
    "v_mfma_f32_16x16x32_bf16 v[40:43], a[248:251], a[188:191], v[40:43]\n"
    "v_add_f32 v6, v232, v6\n"
    "v_add_f32 v7, v232, v7\n"
    "v_mfma_f32_16x16x32_bf16 v[36:39], a[252:255], a[156:159], v[36:39]\n"
    "s_waitcnt lgkmcnt(0)\n"
    "s_barrier\n"
    "v_add_f32 v8, v236, v8\n"
    "v_add_f32 v9, v236, v9\n"
    "v_mfma_f32_16x16x32_bf16 v[44:47], a[252:255], a[188:191], v[44:47]\n"
    "v_add_f32 v10, v236, v10\n"
    "v_add_f32 v11, v236, v11\n"
    "v_add_f32 v12, v236, v12\n"
    "v_add_f32 v13, v236, v13\n"
    "v_add_f32 v14, v236, v14\n"
    "v_add_f32 v15, v236, v15\n"
    "v_exp_f32 v0, v0\n"
    "v_exp_f32 v1, v1\n"
    "v_exp_f32 v2, v2\n"
    "v_add_f32 v200, v200, v0\n"
    "v_cvt_pk_bf16_f32 v144, v0, v1\n"
    "v_exp_f32 v3, v3\n"
    "v_add_f32 v200, v200, v1\n"
    "v_add_f32 v200, v200, v2\n"
    "v_exp_f32 v4, v4\n"
    "v_add_f32 v200, v200, v3\n"
    "v_cvt_pk_bf16_f32 v145, v2, v3\n"
    "v_exp_f32 v5, v5\n"
    "ds_read_b128 v[176:179], v229 offset:512\n"
    "ds_read_b128 v[180:183], v229 offset:768\n"
    "s_add_u32 m0, s65, 12288\n"
    "s_add_u32 s75, s67, 49152\n"
    "buffer_load_dwordx4 v212, s[52:55], s75 offen offset:0 nt lds\n"
    "buffer_load_dwordx4 v213, s[52:55], s75 offen offset:1024 nt lds\n"
    "buffer_load_dwordx4 v214, s[52:55], s75 offen offset:2048 nt lds\n"
    "buffer_load_dwordx4 v215, s[52:55], s75 offen offset:3072 nt lds\n"
    "s_add_u32 s67, s67, 65536\n"
    "s_add_u32 s67, s67, s81\n"
    "s_and_b32 s67, s67, 0x7ffff\n"
    "v_mov_b32 v192, 0\n"
    "v_mov_b32 v193, 0\n"
    "v_mov_b32 v194, 0\n"
    "v_mov_b32 v195, 0\n"
    "v_exp_f32 v6, v6\n"
    "v_add_f32 v200, v200, v4\n"
    "v_cvt_pk_bf16_f32 v146, v4, v5\n"
    "v_exp_f32 v7, v7\n"
    "v_add_f32 v200, v200, v5\n"
    "v_add_f32 v200, v200, v6\n"
    "v_cvt_pk_bf16_f32 v147, v6, v7\n"
    "v_add_f32 v200, v200, v7\n"
    "v_max3_f32 v148, v32, v33, v34\n"
    "v_max3_f32 v149, v40, v41, v42\n"
    "v_max3_f32 v148, v148, v35, v36\n"
    "v_max3_f32 v149, v149, v43, v44\n"
    "v_max3_f32 v148, v148, v37, v38\n"
    "v_max3_f32 v149, v149, v45, v46\n"
    "v_max_f32 v148, v148, v39\n"
    "v_max_f32 v149, v149, v47\n"
    "v_mov_b32 v154, v148\n"
    "v_mov_b32 v155, v149\n"
    "s_nop 0\n"
    "v_permlane32_swap_b32 v148, v154\n"
    "v_permlane32_swap_b32 v149, v155\n"
    "v_max_f32 v148, v148, v154\n"
    "v_max_f32 v149, v149, v155\n"
    "v_mov_b32 v154, v148\n"
    "v_mov_b32 v155, v149\n"
    "s_nop 0\n"
    "v_permlane16_swap_b32 v148, v154\n"
    "v_permlane16_swap_b32 v149, v155\n"
    "v_max_f32 v148, v148, v154\n"
    "v_max_f32 v149, v149, v155\n"
    "ds_write_b32 v228, v148 offset:1024\n"
    "ds_write_b32 v228, v149 offset:1280\n"
    "ds_write_b128 v222, v[144:147] offset:0\n"
    "s_waitcnt lgkmcnt(4)\n"
    "v_max3_f32 v146, v176, v177, v178\n"
    "s_waitcnt lgkmcnt(3)\n"
    "v_max3_f32 v147, v180, v181, v182\n"
    "v_max_f32 v146, v146, v179\n"
    "v_max_f32 v147, v147, v183\n"
    "v_sub_f32 v240, 0, v146\n"
    "v_sub_f32 v241, 0, v146\n"
    "v_sub_f32 v242, 0, v146\n"
    "v_sub_f32 v243, 0, v146\n"
    "v_sub_f32 v244, 0, v147\n"
    "v_sub_f32 v245, 0, v147\n"
    "v_sub_f32 v246, 0, v147\n"
    "v_sub_f32 v247, 0, v147\n"
    "v_pk_add_f32 v[16:17], v[240:241], v[16:17]\n"
    "v_pk_add_f32 v[18:19], v[240:241], v[18:19]\n"
    "v_pk_add_f32 v[20:21], v[240:241], v[20:21]\n"
    "v_pk_add_f32 v[22:23], v[240:241], v[22:23]\n"
    "v_pk_add_f32 v[24:25], v[244:245], v[24:25]\n"
    "v_pk_add_f32 v[26:27], v[244:245], v[26:27]\n"
    "v_pk_add_f32 v[28:29], v[244:245], v[28:29]\n"
    "v_pk_add_f32 v[30:31], v[244:245], v[30:31]\n"
    "v_exp_f32 v8, v8\n"
    "v_exp_f32 v9, v9\n"
    "v_mfma_f32_16x16x32_bf16 v[48:51], v[192:195], v[192:195], 0\n"
    "v_exp_f32 v10, v10\n"
    "v_add_f32 v201, v201, v8\n"
    "v_mfma_f32_16x16x32_bf16 v[52:55], v[192:195], v[192:195], 0\n"
    "v_cvt_pk_bf16_f32 v148, v8, v9\n"
    "v_exp_f32 v11, v11\n"
    "v_mfma_f32_16x16x32_bf16 v[56:59], v[192:195], v[192:195], 0\n"
    "v_add_f32 v201, v201, v9\n"
    "v_add_f32 v201, v201, v10\n"
    "v_mfma_f32_16x16x32_bf16 v[60:63], v[192:195], v[192:195], 0\n"
    "v_exp_f32 v12, v12\n"
    "v_add_f32 v201, v201, v11\n"
    "v_mfma_f32_16x16x32_bf16 v[64:67], v[192:195], v[192:195], 0\n"
    "v_cvt_pk_bf16_f32 v149, v10, v11\n"
    "v_exp_f32 v13, v13\n"
    "v_mfma_f32_16x16x32_bf16 v[68:71], v[192:195], v[192:195], 0\n"
    "v_exp_f32 v14, v14\n"
    "v_add_f32 v201, v201, v12\n"
    "v_mfma_f32_16x16x32_bf16 v[72:75], v[192:195], v[192:195], 0\n"
    "v_cvt_pk_bf16_f32 v150, v12, v13\n"
    "v_exp_f32 v15, v15\n"
    "v_mfma_f32_16x16x32_bf16 v[76:79], v[192:195], v[192:195], 0\n"
    "v_add_f32 v201, v201, v13\n"
    "v_add_f32 v201, v201, v14\n"
    "v_mfma_f32_16x16x32_bf16 v[80:83], v[192:195], v[192:195], 0\n"
    "v_cvt_pk_bf16_f32 v151, v14, v15\n"
    "v_add_f32 v201, v201, v15\n"
    "v_mfma_f32_16x16x32_bf16 v[84:87], v[192:195], v[192:195], 0\n"
    "ds_write_b128 v222, v[148:151] offset:1024\n"
    "s_waitcnt lgkmcnt(2)\n"
    "s_barrier\n"
    "ds_read_b128 v[184:187], v229 offset:1024\n"
    "ds_read_b128 v[188:191], v229 offset:1280\n"
    "s_sub_u32 s88, s64, s84\n"
    "s_add_u32 m0, s88, 159744\n"
    "s_nop 0\n"
    "buffer_load_dword v206, s[56:59], s82 offen lds\n"
    "s_waitcnt vmcnt(18)\n"
    "ds_read_b128 a[192:195], v216 offset:0\n"
    "ds_read_b128 a[196:199], v216 offset:2048\n"
    "ds_read_b128 a[200:203], v217 offset:0\n"
    "ds_read_b128 a[204:207], v217 offset:2048\n"
    "v_exp_f32 v16, v16\n"
    "v_exp_f32 v17, v17\n"
    "v_mfma_f32_16x16x32_bf16 v[88:91], v[192:195], v[192:195], 0\n"
    "v_exp_f32 v18, v18\n"
    "v_add_f32 v202, v202, v16\n"
    "v_mfma_f32_16x16x32_bf16 v[92:95], v[192:195], v[192:195], 0\n"
    "v_cvt_pk_bf16_f32 v152, v16, v17\n"
    "v_exp_f32 v19, v19\n"
    "v_mfma_f32_16x16x32_bf16 v[96:99], v[192:195], v[192:195], 0\n"
    "v_add_f32 v202, v202, v17\n"
    "v_add_f32 v202, v202, v18\n"
    "v_mfma_f32_16x16x32_bf16 v[100:103], v[192:195], v[192:195], 0\n"
    "v_exp_f32 v20, v20\n"
    "v_add_f32 v202, v202, v19\n"
    "v_mfma_f32_16x16x32_bf16 v[104:107], v[192:195], v[192:195], 0\n"
    "v_cvt_pk_bf16_f32 v153, v18, v19\n"
    "v_exp_f32 v21, v21\n"
    "v_mfma_f32_16x16x32_bf16 v[108:111], v[192:195], v[192:195], 0\n"
    "v_exp_f32 v22, v22\n"
    "v_add_f32 v202, v202, v20\n"
    "v_mfma_f32_16x16x32_bf16 v[112:115], v[192:195], v[192:195], 0\n"
    "v_cvt_pk_bf16_f32 v154, v20, v21\n"
    "v_exp_f32 v23, v23\n"
    "v_mfma_f32_16x16x32_bf16 v[116:119], v[192:195], v[192:195], 0\n"
    "v_add_f32 v202, v202, v21\n"
    "v_add_f32 v202, v202, v22\n"
    "v_mfma_f32_16x16x32_bf16 v[120:123], v[192:195], v[192:195], 0\n"
    "v_cvt_pk_bf16_f32 v155, v22, v23\n"
    "v_add_f32 v202, v202, v23\n"
    "v_mfma_f32_16x16x32_bf16 v[124:127], v[192:195], v[192:195], 0\n"
    "ds_write_b128 v222, v[152:155] offset:2048\n"
    "v_exp_f32 v24, v24\n"
    "v_exp_f32 v25, v25\n"
    "v_mfma_f32_16x16x32_bf16 v[128:131], v[192:195], v[192:195], 0\n"
    "v_exp_f32 v26, v26\n"
    "v_add_f32 v203, v203, v24\n"
    "v_mfma_f32_16x16x32_bf16 v[132:135], v[192:195], v[192:195], 0\n"
    "v_cvt_pk_bf16_f32 v156, v24, v25\n"
    "v_exp_f32 v27, v27\n"
    "v_mfma_f32_16x16x32_bf16 v[136:139], v[192:195], v[192:195], 0\n"
    "v_add_f32 v203, v203, v25\n"
    "v_add_f32 v203, v203, v26\n"
    "v_mfma_f32_16x16x32_bf16 v[140:143], v[192:195], v[192:195], 0\n"
    "v_exp_f32 v28, v28\n"
    "v_add_f32 v203, v203, v27\n"
    "v_cvt_pk_bf16_f32 v157, v26, v27\n"
    "v_exp_f32 v29, v29\n"
    "v_exp_f32 v30, v30\n"
    "v_add_f32 v203, v203, v28\n"
    "v_cvt_pk_bf16_f32 v158, v28, v29\n"
    "v_exp_f32 v31, v31\n"
    "v_add_f32 v203, v203, v29\n"
    "v_add_f32 v203, v203, v30\n"
    "v_cvt_pk_bf16_f32 v159, v30, v31\n"
    "v_add_f32 v203, v203, v31\n"
    "s_waitcnt lgkmcnt(6)\n"
    "v_max3_f32 v148, v184, v185, v186\n"
    "s_waitcnt lgkmcnt(5)\n"
    "v_max3_f32 v149, v188, v189, v190\n"
    "v_max_f32 v148, v148, v187\n"
    "v_max_f32 v149, v149, v191\n"
    "v_sub_f32 v248, 0, v148\n"
    "v_sub_f32 v249, 0, v148\n"
    "v_sub_f32 v250, 0, v148\n"
    "v_sub_f32 v251, 0, v148\n"
    "v_sub_f32 v252, 0, v149\n"
    "v_sub_f32 v253, 0, v149\n"
    "v_sub_f32 v254, 0, v149\n"
    "v_sub_f32 v255, 0, v149\n"
    "v_pk_add_f32 v[32:33], v[248:249], v[32:33]\n"
    "v_pk_add_f32 v[34:35], v[248:249], v[34:35]\n"
    "v_pk_add_f32 v[36:37], v[248:249], v[36:37]\n"
    "v_pk_add_f32 v[38:39], v[248:249], v[38:39]\n"
    "v_pk_add_f32 v[40:41], v[252:253], v[40:41]\n"
    "v_pk_add_f32 v[42:43], v[252:253], v[42:43]\n"
    "v_pk_add_f32 v[44:45], v[252:253], v[44:45]\n"
    "v_pk_add_f32 v[46:47], v[252:253], v[46:47]\n"
    "s_mov_b32 s69, 1\n"
    "s_waitcnt vmcnt(5)\n"
    "LA_%=:\n"
    "s_waitcnt lgkmcnt(4)\n"
    "v_mfma_f32_16x16x32_bf16 v[0:3], a[192:195], a[0:3], v[232:235]\n"
    "s_waitcnt vmcnt(24)\n"
    "ds_read_b128 a[208:211], v216 offset:4096\n"
    "ds_read_b128 a[212:215], v216 offset:6144\n"
    "v_mfma_f32_16x16x32_bf16 v[8:11], a[192:195], a[32:35], v[236:239]\n"
    "ds_write_b128 v222, v[156:159] offset:3072\n"
    "v_mfma_f32_16x16x32_bf16 v[16:19], a[192:195], a[64:67], v[240:243]\n"
    "v_exp_f32 v32, v32\n"
    "v_mfma_f32_16x16x32_bf16 v[24:27], a[192:195], a[96:99], v[244:247]\n"
    "s_waitcnt lgkmcnt(6)\n"
    "v_exp_f32 v33, v33\n"
    "v_mfma_f32_16x16x32_bf16 v[4:7], a[196:199], a[0:3], v[232:235]\n"
    "v_exp_f32 v34, v34\n"
    "v_mfma_f32_16x16x32_bf16 v[12:15], a[196:199], a[32:35], v[236:239]\n"
    "ds_read_b128 a[216:219], v217 offset:4096\n"
    "v_add_f32 v204, v204, v32\n"
    "v_mfma_f32_16x16x32_bf16 v[20:23], a[196:199], a[64:67], v[240:243]\n"
    "ds_read_b128 a[220:223], v217 offset:6144\n"
    "v_cvt_pk_bf16_f32 v160, v32, v33\n"
    "v_mfma_f32_16x16x32_bf16 v[28:31], a[196:199], a[96:99], v[244:247]\n"
    "s_waitcnt lgkmcnt(7)\n"
    "v_exp_f32 v35, v35\n"
    "v_mfma_f32_16x16x32_bf16 v[0:3], a[200:203], a[4:7], v[0:3]\n"
    "s_add_u32 m0, s64, 0\n"
    "v_add_f32 v204, v204, v33\n"
    "v_add_f32 v204, v204, v34\n"
    "v_mfma_f32_16x16x32_bf16 v[8:11], a[200:203], a[36:39], v[8:11]\n"
    "s_waitcnt lgkmcnt(6)\n"
    "buffer_load_dwordx4 v208, s[48:51], s66 offen offset:0 nt lds\n"
    "v_mfma_f32_16x16x32_bf16 v[16:19], a[200:203], a[68:71], v[16:19]\n"
    "v_exp_f32 v36, v36\n"
    "v_mfma_f32_16x16x32_bf16 v[24:27], a[200:203], a[100:103], v[24:27]\n"
    "v_add_f32 v204, v204, v35\n"
    "v_cvt_pk_bf16_f32 v161, v34, v35\n"
    "v_mfma_f32_16x16x32_bf16 v[4:7], a[204:207], a[4:7], v[4:7]\n"
    "v_exp_f32 v37, v37\n"
    "v_mfma_f32_16x16x32_bf16 v[12:15], a[204:207], a[36:39], v[12:15]\n"
    "v_exp_f32 v38, v38\n"
    "v_mfma_f32_16x16x32_bf16 v[20:23], a[204:207], a[68:71], v[20:23]\n"
    "s_waitcnt vmcnt(21)\n"
    "ds_read_b128 a[224:227], v216 offset:8192\n"
    "v_add_f32 v204, v204, v36\n"
    "v_mfma_f32_16x16x32_bf16 v[28:31], a[204:207], a[100:103], v[28:31]\n"
    "s_waitcnt lgkmcnt(5)\n"
    "buffer_load_dwordx4 v209, s[48:51], s66 offen offset:1024 nt lds\n"
    "v_mfma_f32_16x16x32_bf16 v[0:3], a[208:211], a[8:11], v[0:3]\n"
    "ds_read_b128 a[228:231], v216 offset:10240\n"
    "v_cvt_pk_bf16_f32 v162, v36, v37\n"
    "v_mfma_f32_16x16x32_bf16 v[8:11], a[208:211], a[40:43], v[8:11]\n"
    "v_exp_f32 v39, v39\n"
    "v_mfma_f32_16x16x32_bf16 v[16:19], a[208:211], a[72:75], v[16:19]\n"
    "v_add_f32 v204, v204, v37\n"
    "v_add_f32 v204, v204, v38\n"
    "v_mfma_f32_16x16x32_bf16 v[24:27], a[208:211], a[104:107], v[24:27]\n"
    "s_waitcnt lgkmcnt(5)\n"
    "v_cvt_pk_bf16_f32 v163, v38, v39\n"
    "v_add_f32 v204, v204, v39\n"
    "v_mfma_f32_16x16x32_bf16 v[4:7], a[212:215], a[8:11], v[4:7]\n"
    "ds_read_b128 a[232:235], v217 offset:8192\n"
    "v_mfma_f32_16x16x32_bf16 v[12:15], a[212:215], a[40:43], v[12:15]\n"
    "buffer_load_dwordx4 v210, s[48:51], s66 offen offset:2048 nt lds\n"
    "v_mfma_f32_16x16x32_bf16 v[20:23], a[212:215], a[72:75], v[20:23]\n"
    "ds_read_b128 a[236:239], v217 offset:10240\n"
    "v_mfma_f32_16x16x32_bf16 v[28:31], a[212:215], a[104:107], v[28:31]\n"
    "s_waitcnt lgkmcnt(5)\n"
    "ds_write_b128 v222, v[160:163] offset:4096\n"
    "v_mfma_f32_16x16x32_bf16 v[0:3], a[216:219], a[12:15], v[0:3]\n"
    "v_exp_f32 v40, v40\n"
    "v_mfma_f32_16x16x32_bf16 v[8:11], a[216:219], a[44:47], v[8:11]\n"
    "v_exp_f32 v41, v41\n"
    "v_mfma_f32_16x16x32_bf16 v[16:19], a[216:219], a[76:79], v[16:19]\n"
    "v_exp_f32 v42, v42\n"
    "v_mfma_f32_16x16x32_bf16 v[24:27], a[216:219], a[108:111], v[24:27]\n"
    "s_waitcnt lgkmcnt(5)\n"
    "buffer_load_dwordx4 v211, s[48:51], s66 offen offset:3072 nt lds\n"
    "v_mfma_f32_16x16x32_bf16 v[4:7], a[220:223], a[12:15], v[4:7]\n"
    "v_add_f32 v205, v205, v40\n"
    "v_cvt_pk_bf16_f32 v164, v40, v41\n"
    "v_mfma_f32_16x16x32_bf16 v[12:15], a[220:223], a[44:47], v[12:15]\n"
    "v_exp_f32 v43, v43\n"
    "v_mfma_f32_16x16x32_bf16 v[20:23], a[220:223], a[76:79], v[20:23]\n"
    "v_add_f32 v205, v205, v41\n"
    "v_add_f32 v205, v205, v42\n"
    "v_mfma_f32_16x16x32_bf16 v[28:31], a[220:223], a[108:111], v[28:31]\n"
    "s_waitcnt lgkmcnt(4)\n"
    "v_exp_f32 v44, v44\n"
    "v_mfma_f32_16x16x32_bf16 v[0:3], a[224:227], a[16:19], v[0:3]\n"
    "s_add_u32 s66, s66, s68\n"
    "s_add_u32 m0, s64, 4096\n"
    "s_waitcnt vmcnt(20)\n"
    "ds_read_b128 a[240:243], v216 offset:12288\n"
    "v_add_f32 v205, v205, v43\n"
    "v_mfma_f32_16x16x32_bf16 v[8:11], a[224:227], a[48:51], v[8:11]\n"
    "buffer_load_dwordx4 v208, s[48:51], s66 offen offset:0 nt lds\n"
    "v_mfma_f32_16x16x32_bf16 v[16:19], a[224:227], a[80:83], v[16:19]\n"
    "ds_read_b128 a[244:247], v216 offset:14336\n"
    "v_cvt_pk_bf16_f32 v165, v42, v43\n"
    "v_mfma_f32_16x16x32_bf16 v[24:27], a[224:227], a[112:115], v[24:27]\n"
    "s_waitcnt lgkmcnt(5)\n"
    "v_exp_f32 v45, v45\n"
    "v_mfma_f32_16x16x32_bf16 v[4:7], a[228:231], a[16:19], v[4:7]\n"
    "v_exp_f32 v46, v46\n"
    "v_mfma_f32_16x16x32_bf16 v[12:15], a[228:231], a[48:51], v[12:15]\n"
    "v_add_f32 v205, v205, v44\n"
    "v_cvt_pk_bf16_f32 v166, v44, v45\n"
    "v_mfma_f32_16x16x32_bf16 v[20:23], a[228:231], a[80:83], v[20:23]\n"
    "v_exp_f32 v47, v47\n"
    "v_mfma_f32_16x16x32_bf16 v[28:31], a[228:231], a[112:115], v[28:31]\n"
    "s_waitcnt lgkmcnt(4)\n"
    "v_add_f32 v205, v205, v45\n"
    "v_add_f32 v205, v205, v46\n"
    "v_mfma_f32_16x16x32_bf16 v[0:3], a[232:235], a[20:23], v[0:3]\n"
    "buffer_load_dwordx4 v209, s[48:51], s66 offen offset:1024 nt lds\n"
    "v_mfma_f32_16x16x32_bf16 v[8:11], a[232:235], a[52:55], v[8:11]\n"
    "v_cvt_pk_bf16_f32 v167, v46, v47\n"
    "v_add_f32 v205, v205, v47\n"
    "v_mfma_f32_16x16x32_bf16 v[16:19], a[232:235], a[84:87], v[16:19]\n"
    "ds_write_b128 v222, v[164:167] offset:5120\n"
    "v_mfma_f32_16x16x32_bf16 v[24:27], a[232:235], a[116:119], v[24:27]\n"
    "s_waitcnt lgkmcnt(4)\n"
    "ds_read_b128 a[248:251], v217 offset:12288\n"
    "ds_read_b128 a[252:255], v217 offset:14336\n"
    "v_mfma_f32_16x16x32_bf16 v[4:7], a[236:239], a[20:23], v[4:7]\n"
    "v_mfma_f32_16x16x32_bf16 v[12:15], a[236:239], a[52:55], v[12:15]\n"
    "buffer_load_dwordx4 v210, s[48:51], s66 offen offset:2048 nt lds\n"
    "v_mfma_f32_16x16x32_bf16 v[20:23], a[236:239], a[84:87], v[20:23]\n"
    "v_mfma_f32_16x16x32_bf16 v[28:31], a[236:239], a[116:119], v[28:31]\n"
    "s_waitcnt lgkmcnt(4)\n"
    "v_mfma_f32_16x16x32_bf16 v[0:3], a[240:243], a[24:27], v[0:3]\n"
    "s_waitcnt lgkmcnt(2)\n"
    "s_barrier\n"
    "v_mfma_f32_16x16x32_bf16 v[8:11], a[240:243], a[56:59], v[8:11]\n"
    "v_mfma_f32_16x16x32_bf16 v[16:19], a[240:243], a[88:91], v[16:19]\n"
    "v_mfma_f32_16x16x32_bf16 v[24:27], a[240:243], a[120:123], v[24:27]\n"
    "buffer_load_dwordx4 v211, s[48:51], s66 offen offset:3072 nt lds\n"
    "v_mfma_f32_16x16x32_bf16 v[4:7], a[244:247], a[24:27], v[4:7]\n"
    "s_waitcnt vmcnt(20)\n"
    "ds_read_b64_tr_b16 v[168:169], v218 offset:0\n"
    "v_mfma_f32_16x16x32_bf16 v[12:15], a[244:247], a[56:59], v[12:15]\n"
    "v_mfma_f32_16x16x32_bf16 v[20:23], a[244:247], a[88:91], v[20:23]\n"
    "ds_read_b64_tr_b16 v[170:171], v218 offset:2048\n"
    "v_mfma_f32_16x16x32_bf16 v[28:31], a[244:247], a[120:123], v[28:31]\n"
    "s_waitcnt lgkmcnt(3)\n"
    "v_mfma_f32_16x16x32_bf16 v[0:3], a[248:251], a[28:31], v[0:3]\n"
    "s_add_u32 s66, s66, s68\n"
    "s_add_u32 m0, s64, 8192\n"
    "ds_read_b64_tr_b16 v[172:173], v219 offset:0\n"
    "v_mfma_f32_16x16x32_bf16 v[8:11], a[248:251], a[60:63], v[8:11]\n"
    "buffer_load_dwordx4 v208, s[48:51], s66 offen offset:0 nt lds\n"
    "v_mfma_f32_16x16x32_bf16 v[16:19], a[248:251], a[92:95], v[16:19]\n"
    "ds_read_b64_tr_b16 v[174:175], v219 offset:2048\n"
    "v_mfma_f32_16x16x32_bf16 v[24:27], a[248:251], a[124:127], v[24:27]\n"
    "s_waitcnt lgkmcnt(4)\n"
    "v_mfma_f32_16x16x32_bf16 v[4:7], a[252:255], a[28:31], v[4:7]\n"
    "ds_read_b64_tr_b16 v[176:177], v220 offset:0\n"
    "v_mfma_f32_16x16x32_bf16 v[12:15], a[252:255], a[60:63], v[12:15]\n"
    "v_mfma_f32_16x16x32_bf16 v[20:23], a[252:255], a[92:95], v[20:23]\n"
    "ds_read_b64_tr_b16 v[178:179], v220 offset:2048\n"
    "v_mfma_f32_16x16x32_bf16 v[28:31], a[252:255], a[124:127], v[28:31]\n"
    "buffer_load_dwordx4 v209, s[48:51], s66 offen offset:1024 nt lds\n"
    "v_mfma_f32_16x16x32_bf16 v[32:35], a[192:195], a[128:131], v[248:251]\n"
    "ds_read_b64_tr_b16 v[180:181], v221 offset:0\n"
    "v_mfma_f32_16x16x32_bf16 v[40:43], a[192:195], a[160:163], v[252:255]\n"
    "v_mfma_f32_16x16x32_bf16 v[36:39], a[196:199], a[128:131], v[248:251]\n"
    "ds_read_b64_tr_b16 v[182:183], v221 offset:2048\n"
    "v_mfma_f32_16x16x32_bf16 v[44:47], a[196:199], a[160:163], v[252:255]\n"
    "v_mfma_f32_16x16x32_bf16 v[32:35], a[200:203], a[132:135], v[32:35]\n"
    "v_exp_f32 v184, v0\n"
    "v_mfma_f32_16x16x32_bf16 v[40:43], a[200:203], a[164:167], v[40:43]\n"
    "buffer_load_dwordx4 v210, s[48:51], s66 offen offset:2048 nt lds\n"
    "v_mfma_f32_16x16x32_bf16 v[36:39], a[204:207], a[132:135], v[36:39]\n"
    "v_exp_f32 v185, v1\n"
    "v_mfma_f32_16x16x32_bf16 v[44:47], a[204:207], a[164:167], v[44:47]\n"
    "v_exp_f32 v186, v2\n"
    "v_mfma_f32_16x16x32_bf16 v[32:35], a[208:211], a[136:139], v[32:35]\n"
    "v_add_f32 v200, v200, v184\n"
    "v_mfma_f32_16x16x32_bf16 v[40:43], a[208:211], a[168:171], v[40:43]\n"
    "v_cvt_pk_bf16_f32 v144, v184, v185\n"
    "v_mfma_f32_16x16x32_bf16 v[36:39], a[212:215], a[136:139], v[36:39]\n"
    "v_exp_f32 v187, v3\n"
    "v_mfma_f32_16x16x32_bf16 v[44:47], a[212:215], a[168:171], v[44:47]\n"
    "buffer_load_dwordx4 v211, s[48:51], s66 offen offset:3072 nt lds\n"
    "s_add_u32 s66, s66, s68\n"
    "s_add_u32 m0, s64, 12288\n"
    "v_mfma_f32_16x16x32_bf16 v[32:35], a[216:219], a[140:143], v[32:35]\n"
    "buffer_load_dwordx4 v208, s[48:51], s66 offen offset:0 nt lds\n"
    "v_mfma_f32_16x16x32_bf16 v[40:43], a[216:219], a[172:175], v[40:43]\n"
    "v_add_f32 v200, v200, v185\n"
    "v_add_f32 v200, v200, v186\n"
    "v_mfma_f32_16x16x32_bf16 v[36:39], a[220:223], a[140:143], v[36:39]\n"
    "v_exp_f32 v188, v4\n"
    "v_mfma_f32_16x16x32_bf16 v[44:47], a[220:223], a[172:175], v[44:47]\n"
    "v_add_f32 v200, v200, v187\n"
    "v_cvt_pk_bf16_f32 v145, v186, v187\n"
    "v_mfma_f32_16x16x32_bf16 v[32:35], a[224:227], a[144:147], v[32:35]\n"
    "buffer_load_dwordx4 v209, s[48:51], s66 offen offset:1024 nt lds\n"
    "v_mfma_f32_16x16x32_bf16 v[40:43], a[224:227], a[176:179], v[40:43]\n"
    "v_exp_f32 v189, v5\n"
    "v_mfma_f32_16x16x32_bf16 v[36:39], a[228:231], a[144:147], v[36:39]\n"
    "v_exp_f32 v190, v6\n"
    "v_mfma_f32_16x16x32_bf16 v[44:47], a[228:231], a[176:179], v[44:47]\n"
    "v_add_f32 v200, v200, v188\n"
    "v_mfma_f32_16x16x32_bf16 v[32:35], a[232:235], a[148:151], v[32:35]\n"
    "ds_read_b128 a[208:211], v223 offset:0\n"
    "v_cvt_pk_bf16_f32 v146, v188, v189\n"
    "v_mfma_f32_16x16x32_bf16 v[40:43], a[232:235], a[180:183], v[40:43]\n"
    "v_exp_f32 v191, v7\n"
    "v_mfma_f32_16x16x32_bf16 v[36:39], a[236:239], a[148:151], v[36:39]\n"
    "ds_read_b128 a[212:215], v223 offset:1024\n"
    "v_add_f32 v200, v200, v189\n"
    "v_mfma_f32_16x16x32_bf16 v[44:47], a[236:239], a[180:183], v[44:47]\n"
    "buffer_load_dwordx4 v210, s[48:51], s66 offen offset:2048 nt lds\n"
    "v_mfma_f32_16x16x32_bf16 v[32:35], a[240:243], a[152:155], v[32:35]\n"
    "ds_read_b128 a[216:219], v223 offset:2048\n"
    "v_add_f32 v200, v200, v190\n"
    "v_mfma_f32_16x16x32_bf16 v[40:43], a[240:243], a[184:187], v[40:43]\n"
    "ds_read_b128 a[220:223], v223 offset:3072\n"
    "v_cvt_pk_bf16_f32 v147, v190, v191\n"
    "v_mfma_f32_16x16x32_bf16 v[36:39], a[244:247], a[152:155], v[36:39]\n"
    "ds_read_b128 a[224:227], v223 offset:4096\n"
    "v_add_f32 v200, v200, v191\n"
    "v_mfma_f32_16x16x32_bf16 v[44:47], a[244:247], a[184:187], v[44:47]\n"
    "ds_read_b128 a[228:231], v223 offset:5120\n"
    "v_mfma_f32_16x16x32_bf16 v[32:35], a[248:251], a[156:159], v[32:35]\n"
    "buffer_load_dwordx4 v211, s[48:51], s66 offen offset:3072 nt lds\n"
    "s_add_u32 s66, s66, s68\n"
    "s_add_u32 s66, s66, s80\n"
    "s_and_b32 s66, s66, 0x7ffff\n"
    "v_mfma_f32_16x16x32_bf16 v[40:43], a[248:251], a[188:191], v[40:43]\n"
    "v_mfma_f32_16x16x32_bf16 v[36:39], a[252:255], a[156:159], v[36:39]\n"
    "v_mfma_f32_16x16x32_bf16 v[44:47], a[252:255], a[188:191], v[44:47]\n"
    "s_cmp_eq_u32 s69, 7\n"
    "s_cbranch_scc1 LFIN_%=\n"
    "s_waitcnt lgkmcnt(5)\n"
    "v_mfma_f32_16x16x32_bf16 v[48:51], v[168:171], a[208:211], v[48:51]\n"
    "s_waitcnt vmcnt(24)\n"
    "ds_read_b64_tr_b16 v[184:185], v218 offset:4096\n"
    "v_mfma_f32_16x16x32_bf16 v[52:55], v[172:175], a[208:211], v[52:55]\n"
    "s_add_u32 m0, s65, 0\n"
    "v_exp_f32 v8, v8\n"
    "v_mfma_f32_16x16x32_bf16 v[56:59], v[176:179], a[208:211], v[56:59]\n"
    "buffer_load_dwordx4 v212, s[52:55], s67 offen offset:0 nt lds\n"
    "v_mfma_f32_16x16x32_bf16 v[60:63], v[180:183], a[208:211], v[60:63]\n"
    "s_waitcnt lgkmcnt(5)\n"
    "ds_read_b64_tr_b16 v[186:187], v218 offset:6144\n"
    "ds_read_b64_tr_b16 v[188:189], v219 offset:4096\n"
    "v_mfma_f32_16x16x32_bf16 v[64:67], v[168:171], a[212:215], v[64:67]\n"
    "v_exp_f32 v9, v9\n"
    "v_mfma_f32_16x16x32_bf16 v[68:71], v[172:175], a[212:215], v[68:71]\n"
    "ds_read_b64_tr_b16 v[190:191], v219 offset:6144\n"
    "ds_read_b128 a[232:235], v223 offset:6144\n"
    "v_mfma_f32_16x16x32_bf16 v[72:75], v[176:179], a[212:215], v[72:75]\n"
    "v_exp_f32 v10, v10\n"
    "v_mfma_f32_16x16x32_bf16 v[76:79], v[180:183], a[212:215], v[76:79]\n"
    "s_waitcnt lgkmcnt(8)\n"
    "ds_read_b64_tr_b16 v[192:193], v220 offset:4096\n"
    "ds_read_b64_tr_b16 v[194:195], v220 offset:6144\n"
    "v_mfma_f32_16x16x32_bf16 v[80:83], v[168:171], a[216:219], v[80:83]\n"
    "buffer_load_dwordx4 v213, s[52:55], s67 offen offset:1024 nt lds\n"
    "v_mfma_f32_16x16x32_bf16 v[84:87], v[172:175], a[216:219], v[84:87]\n"
    "ds_read_b64_tr_b16 v[196:197], v221 offset:4096\n"
    "v_add_f32 v201, v201, v8\n"
    "v_mfma_f32_16x16x32_bf16 v[88:91], v[176:179], a[216:219], v[88:91]\n"
    "ds_read_b64_tr_b16 v[198:199], v221 offset:6144\n"
    "v_cvt_pk_bf16_f32 v148, v8, v9\n"
    "v_mfma_f32_16x16x32_bf16 v[92:95], v[180:183], a[216:219], v[92:95]\n"
    "s_waitcnt lgkmcnt(11)\n"
    "v_exp_f32 v11, v11\n"
    "v_mfma_f32_16x16x32_bf16 v[96:99], v[168:171], a[220:223], v[96:99]\n"
    "ds_read_b128 a[236:239], v223 offset:7168\n"
    "v_add_f32 v201, v201, v9\n"
    "v_mfma_f32_16x16x32_bf16 v[100:103], v[172:175], a[220:223], v[100:103]\n"
    "ds_read_b128 a[240:243], v223 offset:8192\n"
    "v_add_f32 v201, v201, v10\n"
    "v_mfma_f32_16x16x32_bf16 v[104:107], v[176:179], a[220:223], v[104:107]\n"
    "buffer_load_dwordx4 v214, s[52:55], s67 offen offset:2048 nt lds\n"
    "v_mfma_f32_16x16x32_bf16 v[108:111], v[180:183], a[220:223], v[108:111]\n"
    "s_waitcnt lgkmcnt(12)\n"
    "v_exp_f32 v12, v12\n"
    "v_mfma_f32_16x16x32_bf16 v[112:115], v[168:171], a[224:227], v[112:115]\n"
    "ds_read_b128 a[244:247], v223 offset:9216\n"
    "ds_read_b128 a[248:251], v223 offset:10240\n"
    "v_mfma_f32_16x16x32_bf16 v[116:119], v[172:175], a[224:227], v[116:119]\n"
    "ds_read_b128 a[252:255], v223 offset:11264\n"
    "v_add_f32 v201, v201, v11\n"
    "v_mfma_f32_16x16x32_bf16 v[120:123], v[176:179], a[224:227], v[120:123]\n"
    "v_cvt_pk_bf16_f32 v149, v10, v11\n"
    "v_mfma_f32_16x16x32_bf16 v[124:127], v[180:183], a[224:227], v[124:127]\n"
    "s_waitcnt lgkmcnt(14)\n"
    "v_exp_f32 v13, v13\n"
    "v_mfma_f32_16x16x32_bf16 v[128:131], v[168:171], a[228:231], v[128:131]\n"
    "buffer_load_dwordx4 v215, s[52:55], s67 offen offset:3072 nt lds\n"
    "v_mfma_f32_16x16x32_bf16 v[132:135], v[172:175], a[228:231], v[132:135]\n"
    "v_exp_f32 v14, v14\n"
    "v_mfma_f32_16x16x32_bf16 v[136:139], v[176:179], a[228:231], v[136:139]\n"
    "v_add_f32 v201, v201, v12\n"
    "v_cvt_pk_bf16_f32 v150, v12, v13\n"
    "v_mfma_f32_16x16x32_bf16 v[140:143], v[180:183], a[228:231], v[140:143]\n"
    "s_waitcnt lgkmcnt(9)\n"
    "v_exp_f32 v15, v15\n"
    "v_mfma_f32_16x16x32_bf16 v[48:51], v[184:187], a[232:235], v[48:51]\n"
    "v_mfma_f32_16x16x32_bf16 v[52:55], v[188:191], a[232:235], v[52:55]\n"
    "s_waitcnt lgkmcnt(7)\n"
    "s_add_u32 s67, s67, 16384\n"
    "s_add_u32 m0, s65, 4096\n"
    "s_waitcnt vmcnt(24)\n"
    "ds_read_b64_tr_b16 v[168:169], v218 offset:8192\n"
    "v_add_f32 v201, v201, v13\n"
    "v_mfma_f32_16x16x32_bf16 v[56:59], v[192:195], a[232:235], v[56:59]\n"
    "s_waitcnt lgkmcnt(6)\n"
    "buffer_load_dwordx4 v212, s[52:55], s67 offen offset:0 nt lds\n"
    "v_mfma_f32_16x16x32_bf16 v[60:63], v[196:199], a[232:235], v[60:63]\n"
    "s_waitcnt lgkmcnt(5)\n"
    "ds_read_b64_tr_b16 v[170:171], v218 offset:10240\n"
    "ds_read_b64_tr_b16 v[172:173], v219 offset:8192\n"
    "v_mfma_f32_16x16x32_bf16 v[64:67], v[184:187], a[236:239], v[64:67]\n"
    "ds_read_b64_tr_b16 v[174:175], v219 offset:10240\n"
    "v_add_f32 v201, v201, v14\n"
    "v_mfma_f32_16x16x32_bf16 v[68:71], v[188:191], a[236:239], v[68:71]\n"
    "ds_read_b128 a[208:211], v223 offset:12288\n"
    "v_cvt_pk_bf16_f32 v151, v14, v15\n"
    "v_mfma_f32_16x16x32_bf16 v[72:75], v[192:195], a[236:239], v[72:75]\n"
    "ds_read_b64_tr_b16 v[176:177], v220 offset:8192\n"
    "v_add_f32 v201, v201, v15\n"
    "v_mfma_f32_16x16x32_bf16 v[76:79], v[196:199], a[236:239], v[76:79]\n"
    "s_waitcnt lgkmcnt(9)\n"
    "v_exp_f32 v16, v16\n"
    "v_mfma_f32_16x16x32_bf16 v[80:83], v[184:187], a[240:243], v[80:83]\n"
    "ds_read_b64_tr_b16 v[178:179], v220 offset:10240\n"
    "ds_read_b64_tr_b16 v[180:181], v221 offset:8192\n"
    "v_mfma_f32_16x16x32_bf16 v[84:87], v[188:191], a[240:243], v[84:87]\n"
    "buffer_load_dwordx4 v213, s[52:55], s67 offen offset:1024 nt lds\n"
    "v_mfma_f32_16x16x32_bf16 v[88:91], v[192:195], a[240:243], v[88:91]\n"
    "v_exp_f32 v17, v17\n"
    "v_mfma_f32_16x16x32_bf16 v[92:95], v[196:199], a[240:243], v[92:95]\n"
    "s_waitcnt lgkmcnt(10)\n"
    "ds_read_b64_tr_b16 v[182:183], v221 offset:10240\n"
    "ds_read_b128 a[212:215], v223 offset:13312\n"
    "v_mfma_f32_16x16x32_bf16 v[96:99], v[184:187], a[244:247], v[96:99]\n"
    "v_exp_f32 v18, v18\n"
    "v_mfma_f32_16x16x32_bf16 v[100:103], v[188:191], a[244:247], v[100:103]\n"
    "ds_read_b128 a[216:219], v223 offset:14336\n"
    "v_add_f32 v202, v202, v16\n"
    "v_mfma_f32_16x16x32_bf16 v[104:107], v[192:195], a[244:247], v[104:107]\n"
    "buffer_load_dwordx4 v214, s[52:55], s67 offen offset:2048 nt lds\n"
    "v_mfma_f32_16x16x32_bf16 v[108:111], v[196:199], a[244:247], v[108:111]\n"
    "s_waitcnt lgkmcnt(12)\n"
    "ds_read_b128 a[220:223], v223 offset:15360\n"
    "v_cvt_pk_bf16_f32 v152, v16, v17\n"
    "v_mfma_f32_16x16x32_bf16 v[112:115], v[184:187], a[248:251], v[112:115]\n"
    "v_exp_f32 v19, v19\n"
    "v_mfma_f32_16x16x32_bf16 v[116:119], v[188:191], a[248:251], v[116:119]\n"
    "ds_read_b128 a[224:227], v223 offset:16384\n"
    "ds_read_b128 a[228:231], v223 offset:17408\n"
    "v_mfma_f32_16x16x32_bf16 v[120:123], v[192:195], a[248:251], v[120:123]\n"
    "v_add_f32 v202, v202, v17\n"
    "v_add_f32 v202, v202, v18\n"
    "v_mfma_f32_16x16x32_bf16 v[124:127], v[196:199], a[248:251], v[124:127]\n"
    "s_waitcnt lgkmcnt(14)\n"
    "buffer_load_dwordx4 v215, s[52:55], s67 offen offset:3072 nt lds\n"
    "v_mfma_f32_16x16x32_bf16 v[128:131], v[184:187], a[252:255], v[128:131]\n"
    "v_exp_f32 v20, v20\n"
    "v_mfma_f32_16x16x32_bf16 v[132:135], v[188:191], a[252:255], v[132:135]\n"
    "v_add_f32 v202, v202, v19\n"
    "v_cvt_pk_bf16_f32 v153, v18, v19\n"
    "v_mfma_f32_16x16x32_bf16 v[136:139], v[192:195], a[252:255], v[136:139]\n"
    "v_exp_f32 v21, v21\n"
    "v_mfma_f32_16x16x32_bf16 v[140:143], v[196:199], a[252:255], v[140:143]\n"
    "s_waitcnt lgkmcnt(9)\n"
    "v_mfma_f32_16x16x32_bf16 v[48:51], v[168:171], a[208:211], v[48:51]\n"
    "v_exp_f32 v22, v22\n"
    "v_mfma_f32_16x16x32_bf16 v[52:55], v[172:175], a[208:211], v[52:55]\n"
    "s_waitcnt lgkmcnt(7)\n"
    "s_add_u32 s67, s67, 16384\n"
    "s_add_u32 m0, s65, 8192\n"
    "s_waitcnt vmcnt(24)\n"
    "ds_read_b64_tr_b16 v[184:185], v218 offset:12288\n"
    "v_mfma_f32_16x16x32_bf16 v[56:59], v[176:179], a[208:211], v[56:59]\n"
    "s_waitcnt lgkmcnt(6)\n"
    "buffer_load_dwordx4 v212, s[52:55], s67 offen offset:0 nt lds\n"
    "v_mfma_f32_16x16x32_bf16 v[60:63], v[180:183], a[208:211], v[60:63]\n"
    "s_waitcnt lgkmcnt(5)\n"
    "ds_read_b64_tr_b16 v[186:187], v218 offset:14336\n"
    "ds_read_b64_tr_b16 v[188:189], v219 offset:12288\n"
    "v_mfma_f32_16x16x32_bf16 v[64:67], v[168:171], a[212:215], v[64:67]\n"
    "ds_read_b64_tr_b16 v[190:191], v219 offset:14336\n"
    "v_add_f32 v202, v202, v20\n"
    "v_mfma_f32_16x16x32_bf16 v[68:71], v[172:175], a[212:215], v[68:71]\n"
    "ds_read_b128 a[232:235], v223 offset:18432\n"
    "v_cvt_pk_bf16_f32 v154, v20, v21\n"
    "v_mfma_f32_16x16x32_bf16 v[72:75], v[176:179], a[212:215], v[72:75]\n"
    "v_exp_f32 v23, v23\n"
    "v_mfma_f32_16x16x32_bf16 v[76:79], v[180:183], a[212:215], v[76:79]\n"
    "s_waitcnt lgkmcnt(8)\n"
    "ds_read_b64_tr_b16 v[192:193], v220 offset:12288\n"
    "ds_read_b64_tr_b16 v[194:195], v220 offset:14336\n"
    "v_mfma_f32_16x16x32_bf16 v[80:83], v[168:171], a[216:219], v[80:83]\n"
    "buffer_load_dwordx4 v213, s[52:55], s67 offen offset:1024 nt lds\n"
    "v_mfma_f32_16x16x32_bf16 v[84:87], v[172:175], a[216:219], v[84:87]\n"
    "ds_read_b64_tr_b16 v[196:197], v221 offset:12288\n"
    "v_add_f32 v202, v202, v21\n"
    "v_mfma_f32_16x16x32_bf16 v[88:91], v[176:179], a[216:219], v[88:91]\n"
    "ds_read_b64_tr_b16 v[198:199], v221 offset:14336\n"
    "v_add_f32 v202, v202, v22\n"
    "v_mfma_f32_16x16x32_bf16 v[92:95], v[180:183], a[216:219], v[92:95]\n"
    "s_waitcnt lgkmcnt(11)\n"
    "v_cvt_pk_bf16_f32 v155, v22, v23\n"
    "v_add_f32 v202, v202, v23\n"
    "v_mfma_f32_16x16x32_bf16 v[96:99], v[168:171], a[220:223], v[96:99]\n"
    "v_exp_f32 v24, v24\n"
    "v_mfma_f32_16x16x32_bf16 v[100:103], v[172:175], a[220:223], v[100:103]\n"
    "ds_read_b128 a[236:239], v223 offset:19456\n"
    "ds_read_b128 a[240:243], v223 offset:20480\n"
    "v_mfma_f32_16x16x32_bf16 v[104:107], v[176:179], a[220:223], v[104:107]\n"
    "buffer_load_dwordx4 v214, s[52:55], s67 offen offset:2048 nt lds\n"
    "v_mfma_f32_16x16x32_bf16 v[108:111], v[180:183], a[220:223], v[108:111]\n"
    "s_waitcnt lgkmcnt(12)\n"
    "v_exp_f32 v25, v25\n"
    "v_mfma_f32_16x16x32_bf16 v[112:115], v[168:171], a[224:227], v[112:115]\n"
    "ds_read_b128 a[244:247], v223 offset:21504\n"
    "ds_read_b128 a[248:251], v223 offset:22528\n"
    "v_mfma_f32_16x16x32_bf16 v[116:119], v[172:175], a[224:227], v[116:119]\n"
    "v_exp_f32 v26, v26\n"
    "v_mfma_f32_16x16x32_bf16 v[120:123], v[176:179], a[224:227], v[120:123]\n"
    "ds_read_b128 a[252:255], v223 offset:23552\n"
    "v_add_f32 v203, v203, v24\n"
    "v_mfma_f32_16x16x32_bf16 v[124:127], v[180:183], a[224:227], v[124:127]\n"
    "s_waitcnt lgkmcnt(14)\n"
    "buffer_load_dwordx4 v215, s[52:55], s67 offen offset:3072 nt lds\n"
    "v_mfma_f32_16x16x32_bf16 v[128:131], v[168:171], a[228:231], v[128:131]\n"
    "v_cvt_pk_bf16_f32 v156, v24, v25\n"
    "v_mfma_f32_16x16x32_bf16 v[132:135], v[172:175], a[228:231], v[132:135]\n"
    "v_exp_f32 v27, v27\n"
    "v_mfma_f32_16x16x32_bf16 v[136:139], v[176:179], a[228:231], v[136:139]\n"
    "v_add_f32 v203, v203, v25\n"
    "v_add_f32 v203, v203, v26\n"
    "v_mfma_f32_16x16x32_bf16 v[140:143], v[180:183], a[228:231], v[140:143]\n"
    "s_waitcnt lgkmcnt(9)\n"
    "v_exp_f32 v28, v28\n"
    "v_mfma_f32_16x16x32_bf16 v[48:51], v[184:187], a[232:235], v[48:51]\n"
    "v_mfma_f32_16x16x32_bf16 v[52:55], v[188:191], a[232:235], v[52:55]\n"
    "s_waitcnt lgkmcnt(7)\n"
    "s_waitcnt lgkmcnt(0)\n"
    "s_barrier\n"
    "s_add_u32 s67, s67, 16384\n"
    "s_add_u32 m0, s65, 12288\n"
    "v_add_f32 v203, v203, v27\n"
    "v_mfma_f32_16x16x32_bf16 v[56:59], v[192:195], a[232:235], v[56:59]\n"
    "buffer_load_dwordx4 v212, s[52:55], s67 offen offset:0 nt lds\n"
    "v_mfma_f32_16x16x32_bf16 v[60:63], v[196:199], a[232:235], v[60:63]\n"
    "s_waitcnt vmcnt(25)\n"
    "ds_read_b128 a[192:195], v216 offset:0\n"
    "v_cvt_pk_bf16_f32 v157, v26, v27\n"
    "v_mfma_f32_16x16x32_bf16 v[64:67], v[184:187], a[236:239], v[64:67]\n"
    "v_exp_f32 v29, v29\n"
    "v_mfma_f32_16x16x32_bf16 v[68:71], v[188:191], a[236:239], v[68:71]\n"
    "v_exp_f32 v30, v30\n"
    "v_mfma_f32_16x16x32_bf16 v[72:75], v[192:195], a[236:239], v[72:75]\n"
    "ds_read_b128 a[196:199], v216 offset:2048\n"
    "ds_read_b128 a[200:203], v217 offset:0\n"
    "v_mfma_f32_16x16x32_bf16 v[76:79], v[196:199], a[236:239], v[76:79]\n"
    "v_add_f32 v203, v203, v28\n"
    "v_mfma_f32_16x16x32_bf16 v[80:83], v[184:187], a[240:243], v[80:83]\n"
    "buffer_load_dwordx4 v213, s[52:55], s67 offen offset:1024 nt lds\n"
    "v_mfma_f32_16x16x32_bf16 v[84:87], v[188:191], a[240:243], v[84:87]\n"
    "ds_read_b128 a[204:207], v217 offset:2048\n"
    "v_cvt_pk_bf16_f32 v158, v28, v29\n"
    "v_mfma_f32_16x16x32_bf16 v[88:91], v[192:195], a[240:243], v[88:91]\n"
    "v_exp_f32 v31, v31\n"
    "v_mfma_f32_16x16x32_bf16 v[92:95], v[196:199], a[240:243], v[92:95]\n"
    "v_add_f32 v203, v203, v29\n"
    "v_mfma_f32_16x16x32_bf16 v[96:99], v[184:187], a[244:247], v[96:99]\n"
    "v_add_f32 v203, v203, v30\n"
    "v_mfma_f32_16x16x32_bf16 v[100:103], v[188:191], a[244:247], v[100:103]\n"
    "v_cvt_pk_bf16_f32 v159, v30, v31\n"
    "v_mfma_f32_16x16x32_bf16 v[104:107], v[192:195], a[244:247], v[104:107]\n"
    "buffer_load_dwordx4 v214, s[52:55], s67 offen offset:2048 nt lds\n"
    "v_mfma_f32_16x16x32_bf16 v[108:111], v[196:199], a[244:247], v[108:111]\n"
    "v_add_f32 v203, v203, v31\n"
    "v_mfma_f32_16x16x32_bf16 v[112:115], v[184:187], a[248:251], v[112:115]\n"
    "ds_write_b128 v222, v[144:147] offset:0\n"
    "v_mfma_f32_16x16x32_bf16 v[116:119], v[188:191], a[248:251], v[116:119]\n"
    "ds_write_b128 v222, v[148:151] offset:1024\n"
    "v_mfma_f32_16x16x32_bf16 v[120:123], v[192:195], a[248:251], v[120:123]\n"
    "ds_write_b128 v222, v[152:155] offset:2048\n"
    "v_mfma_f32_16x16x32_bf16 v[124:127], v[196:199], a[248:251], v[124:127]\n"
    "buffer_load_dwordx4 v215, s[52:55], s67 offen offset:3072 nt lds\n"
    "s_add_u32 s67, s67, 16384\n"
    "s_add_u32 s67, s67, s81\n"
    "s_and_b32 s67, s67, 0x7ffff\n"
    "v_mfma_f32_16x16x32_bf16 v[128:131], v[184:187], a[252:255], v[128:131]\n"
    "v_mfma_f32_16x16x32_bf16 v[132:135], v[188:191], a[252:255], v[132:135]\n"
    "v_mfma_f32_16x16x32_bf16 v[136:139], v[192:195], a[252:255], v[136:139]\n"
    "v_mfma_f32_16x16x32_bf16 v[140:143], v[196:199], a[252:255], v[140:143]\n"
    "s_add_u32 s69, s69, 1\n"
    "s_cmp_lt_u32 s69, 7\n"
    "s_cbranch_scc1 LA_%=\n"
    "v_mbcnt_lo_u32_b32 v0, -1, 0\n"
    "v_mbcnt_hi_u32_b32 v0, -1, v0\n"
    "v_and_b32 v228, 15, v0\n"
    "v_lshlrev_b32 v228, 8, v228\n"
    "v_lshl_add_u32 v229, v0, 24, s94\n"
    "buffer_load_dword v225, v228, s[96:99], s87 offen sc1\n"
    "v_lshrrev_b32 v0, 3, v0\n"
    "v_mul_u32_u24 v0, 384, v0\n"
    "v_subrev_u32 v0, s83, v0\n"
    "v_add_u32 v208, v212, v0\n"
    "v_add_u32 v209, v213, v0\n"
    "v_add_u32 v210, v214, v0\n"
    "v_add_u32 v211, v215, v0\n"
    "v_add_u32 v209, 3072, v209\n"
    "v_add_u32 v210, 6144, v210\n"
    "v_add_u32 v211, 9216, v211\n"
    "s_mov_b32 s48, s56\n"
    "s_mov_b32 s49, s57\n"
    "s_mov_b32 s50, s58\n"
    "s_mov_b32 s51, s59\n"
    "s_mov_b32 s66, s76\n"
    "s_mov_b32 s68, 28672\n"
    "s_sub_u32 s88, s64, s84\n"
    "s_add_u32 m0, s88, 155648\n"
    "s_nop 0\n"
    "s_branch LA_%=\n"
    "LFIN_%=:\n"
    "s_waitcnt lgkmcnt(5)\n"
    "v_mfma_f32_16x16x32_bf16 v[48:51], v[168:171], a[208:211], v[48:51]\n"
    "s_add_u32 m0, s65, 0\n"
    "s_waitcnt vmcnt(25)\n"
    "ds_read_b64_tr_b16 v[184:185], v218 offset:4096\n"
    "v_exp_f32 v8, v8\n"
    "v_mfma_f32_16x16x32_bf16 v[52:55], v[172:175], a[208:211], v[52:55]\n"
    "buffer_load_dwordx4 v212, s[52:55], s67 offen offset:0 nt lds\n"
    "ds_read_b64_tr_b16 v[186:187], v218 offset:6144\n"
    "v_mfma_f32_16x16x32_bf16 v[56:59], v[176:179], a[208:211], v[56:59]\n"
    "s_waitcnt vmcnt(1)\n"
    "s_barrier\n"
    "ds_read_b64_tr_b16 v[188:189], v219 offset:4096\n"
    "v_exp_f32 v9, v9\n"
    "v_mfma_f32_16x16x32_bf16 v[60:63], v[180:183], a[208:211], v[60:63]\n"
    "s_waitcnt lgkmcnt(7)\n"
    "ds_read_b64_tr_b16 v[190:191], v219 offset:6144\n"
    "v_exp_f32 v10, v10\n"
    "v_mfma_f32_16x16x32_bf16 v[64:67], v[168:171], a[212:215], v[64:67]\n"
    "ds_read_b64_tr_b16 v[192:193], v220 offset:4096\n"
    "v_add_f32 v201, v201, v8\n"
    "v_cvt_pk_bf16_f32 v148, v8, v9\n"
    "v_mfma_f32_16x16x32_bf16 v[68:71], v[172:175], a[212:215], v[68:71]\n"
    "buffer_load_dwordx4 v213, s[52:55], s67 offen offset:1024 nt lds\n"
    "ds_read_b64_tr_b16 v[194:195], v220 offset:6144\n"
    "v_mfma_f32_16x16x32_bf16 v[72:75], v[176:179], a[212:215], v[72:75]\n"
    "ds_read_b64_tr_b16 v[196:197], v221 offset:4096\n"
    "v_exp_f32 v11, v11\n"
    "v_mfma_f32_16x16x32_bf16 v[76:79], v[180:183], a[212:215], v[76:79]\n"
    "s_waitcnt lgkmcnt(10)\n"
    "ds_read_b64_tr_b16 v[198:199], v221 offset:6144\n"
    "v_add_f32 v201, v201, v9\n"
    "v_add_f32 v201, v201, v10\n"
    "v_mfma_f32_16x16x32_bf16 v[80:83], v[168:171], a[216:219], v[80:83]\n"
    "v_exp_f32 v12, v12\n"
    "v_add_f32 v201, v201, v11\n"
    "v_mfma_f32_16x16x32_bf16 v[84:87], v[172:175], a[216:219], v[84:87]\n"
    "buffer_load_dwordx4 v214, s[52:55], s67 offen offset:2048 nt lds\n"
    "ds_read_b128 a[232:235], v223 offset:6144\n"
    "v_mfma_f32_16x16x32_bf16 v[88:91], v[176:179], a[216:219], v[88:91]\n"
    "v_cvt_pk_bf16_f32 v149, v10, v11\n"
    "v_exp_f32 v13, v13\n"
    "v_mfma_f32_16x16x32_bf16 v[92:95], v[180:183], a[216:219], v[92:95]\n"
    "s_waitcnt lgkmcnt(11)\n"
    "ds_read_b128 a[236:239], v223 offset:7168\n"
    "v_exp_f32 v14, v14\n"
    "v_mfma_f32_16x16x32_bf16 v[96:99], v[168:171], a[220:223], v[96:99]\n"
    "ds_read_b128 a[240:243], v223 offset:8192\n"
    "ds_read_b128 a[244:247], v223 offset:9216\n"
    "v_add_f32 v201, v201, v12\n"
    "v_mfma_f32_16x16x32_bf16 v[100:103], v[172:175], a[220:223], v[100:103]\n"
    "buffer_load_dwordx4 v215, s[52:55], s67 offen offset:3072 nt lds\n"
    "v_cvt_pk_bf16_f32 v150, v12, v13\n"
    "v_mfma_f32_16x16x32_bf16 v[104:107], v[176:179], a[220:223], v[104:107]\n"
    "ds_read_b128 a[248:251], v223 offset:10240\n"
    "v_exp_f32 v15, v15\n"
    "v_mfma_f32_16x16x32_bf16 v[108:111], v[180:183], a[220:223], v[108:111]\n"
    "s_waitcnt lgkmcnt(14)\n"
    "ds_read_b64_tr_b16 a[0:1], v226 offset:0\n"
    "v_add_f32 v201, v201, v13\n"
    "v_add_f32 v201, v201, v14\n"
    "v_mfma_f32_16x16x32_bf16 v[112:115], v[168:171], a[224:227], v[112:115]\n"
    "ds_read_b128 a[252:255], v223 offset:11264\n"
    "v_cvt_pk_bf16_f32 v151, v14, v15\n"
    "v_add_f32 v201, v201, v15\n"
    "v_mfma_f32_16x16x32_bf16 v[116:119], v[172:175], a[224:227], v[116:119]\n"
    "ds_read_b64_tr_b16 a[2:3], v226 offset:2048\n"
    "v_exp_f32 v16, v16\n"
    "v_mfma_f32_16x16x32_bf16 v[120:123], v[176:179], a[224:227], v[120:123]\n"
    "ds_read_b64_tr_b16 a[4:5], v226 offset:4096\n"
    "v_exp_f32 v17, v17\n"
    "v_mfma_f32_16x16x32_bf16 v[124:127], v[180:183], a[224:227], v[124:127]\n"
    "s_waitcnt lgkmcnt(15)\n"
    "s_add_u32 s67, s67, 16384\n"
    "s_add_u32 m0, s65, 4096\n"
    "ds_read_b64_tr_b16 a[6:7], v226 offset:6144\n"
    "ds_read_b64_tr_b16 a[8:9], v226 offset:8192\n"
    "ds_read_b64_tr_b16 a[10:11], v226 offset:10240\n"
    "v_mfma_f32_16x16x32_bf16 v[128:131], v[168:171], a[228:231], v[128:131]\n"
    "s_waitcnt lgkmcnt(12)\n"
    "buffer_load_dwordx4 v212, s[52:55], s67 offen offset:0 nt lds\n"
    "ds_read_b64_tr_b16 a[12:13], v226 offset:12288\n"
    "v_mfma_f32_16x16x32_bf16 v[132:135], v[172:175], a[228:231], v[132:135]\n"
    "v_exp_f32 v18, v18\n"
    "v_add_f32 v202, v202, v16\n"
    "v_mfma_f32_16x16x32_bf16 v[136:139], v[176:179], a[228:231], v[136:139]\n"
    "v_cvt_pk_bf16_f32 v152, v16, v17\n"
    "v_exp_f32 v19, v19\n"
    "v_mfma_f32_16x16x32_bf16 v[140:143], v[180:183], a[228:231], v[140:143]\n"
    "s_waitcnt lgkmcnt(12)\n"
    "ds_read_b64_tr_b16 a[14:15], v226 offset:14336\n"
    "ds_read_b64_tr_b16 a[16:17], v226 offset:16384\n"
    "v_add_f32 v202, v202, v17\n"
    "v_mfma_f32_16x16x32_bf16 v[48:51], v[184:187], a[232:235], v[48:51]\n"
    "buffer_load_dwordx4 v213, s[52:55], s67 offen offset:1024 nt lds\n"
    "ds_read_b64_tr_b16 a[128:129], v218 offset:8192\n"
    "v_mfma_f32_16x16x32_bf16 v[52:55], v[188:191], a[232:235], v[52:55]\n"
    "ds_read_b64_tr_b16 a[130:131], v218 offset:10240\n"
    "ds_read_b64_tr_b16 a[18:19], v226 offset:18432\n"
    "v_add_f32 v202, v202, v18\n"
    "v_mfma_f32_16x16x32_bf16 v[56:59], v[192:195], a[232:235], v[56:59]\n"
    "ds_read_b64_tr_b16 a[132:133], v219 offset:8192\n"
    "v_exp_f32 v20, v20\n"
    "v_mfma_f32_16x16x32_bf16 v[60:63], v[196:199], a[232:235], v[60:63]\n"
    "s_waitcnt lgkmcnt(15)\n"
    "ds_read_b64_tr_b16 a[134:135], v219 offset:10240\n"
    "v_add_f32 v202, v202, v19\n"
    "v_cvt_pk_bf16_f32 v153, v18, v19\n"
    "v_mfma_f32_16x16x32_bf16 v[64:67], v[184:187], a[236:239], v[64:67]\n"
    "buffer_load_dwordx4 v214, s[52:55], s67 offen offset:2048 nt lds\n"
    "ds_read_b64_tr_b16 a[136:137], v220 offset:8192\n"
    "v_mfma_f32_16x16x32_bf16 v[68:71], v[188:191], a[236:239], v[68:71]\n"
    "ds_read_b64_tr_b16 a[138:139], v220 offset:10240\n"
    "v_exp_f32 v21, v21\n"
    "v_mfma_f32_16x16x32_bf16 v[72:75], v[192:195], a[236:239], v[72:75]\n"
    "ds_read_b64_tr_b16 a[140:141], v221 offset:8192\n"
    "v_exp_f32 v22, v22\n"
    "v_mfma_f32_16x16x32_bf16 v[76:79], v[196:199], a[236:239], v[76:79]\n"
    "ds_read_b64_tr_b16 a[142:143], v221 offset:10240\n"
    "v_add_f32 v202, v202, v20\n"
    "v_cvt_pk_bf16_f32 v154, v20, v21\n"
    "v_mfma_f32_16x16x32_bf16 v[80:83], v[184:187], a[240:243], v[80:83]\n"
    "buffer_load_dwordx4 v215, s[52:55], s67 offen offset:3072 nt lds\n"
    "ds_read_b128 a[208:211], v223 offset:12288\n"
    "v_mfma_f32_16x16x32_bf16 v[84:87], v[188:191], a[240:243], v[84:87]\n"
    "v_exp_f32 v23, v23\n"
    "v_add_f32 v202, v202, v21\n"
    "v_mfma_f32_16x16x32_bf16 v[88:91], v[192:195], a[240:243], v[88:91]\n"
    "v_add_f32 v202, v202, v22\n"
    "v_cvt_pk_bf16_f32 v155, v22, v23\n"
    "v_add_f32 v202, v202, v23\n"
    "v_mfma_f32_16x16x32_bf16 v[92:95], v[196:199], a[240:243], v[92:95]\n"
    "ds_read_b128 a[212:215], v223 offset:13312\n"
    "v_exp_f32 v24, v24\n"
    "v_mfma_f32_16x16x32_bf16 v[96:99], v[184:187], a[244:247], v[96:99]\n"
    "ds_read_b128 a[216:219], v223 offset:14336\n"
    "v_exp_f32 v25, v25\n"
    "v_mfma_f32_16x16x32_bf16 v[100:103], v[188:191], a[244:247], v[100:103]\n"
    "ds_read_b128 a[220:223], v223 offset:15360\n"
    "v_exp_f32 v26, v26\n"
    "v_mfma_f32_16x16x32_bf16 v[104:107], v[192:195], a[244:247], v[104:107]\n"
    "ds_read_b128 a[224:227], v223 offset:16384\n"
    "ds_read_b64_tr_b16 a[20:21], v226 offset:20480\n"
    "v_add_f32 v203, v203, v24\n"
    "v_mfma_f32_16x16x32_bf16 v[108:111], v[196:199], a[244:247], v[108:111]\n"
    "s_waitcnt lgkmcnt(15)\n"
    "ds_read_b64_tr_b16 a[22:23], v226 offset:22528\n"
    "ds_read_b64_tr_b16 a[24:25], v226 offset:24576\n"
    "v_cvt_pk_bf16_f32 v156, v24, v25\n"
    "v_mfma_f32_16x16x32_bf16 v[112:115], v[184:187], a[248:251], v[112:115]\n"
    "ds_read_b128 a[228:231], v223 offset:17408\n"
    "v_exp_f32 v27, v27\n"
    "v_mfma_f32_16x16x32_bf16 v[116:119], v[188:191], a[248:251], v[116:119]\n"
    "ds_read_b64_tr_b16 a[26:27], v226 offset:26624\n"
    "ds_read_b64_tr_b16 a[28:29], v226 offset:28672\n"
    "v_add_f32 v203, v203, v25\n"
    "v_mfma_f32_16x16x32_bf16 v[120:123], v[192:195], a[248:251], v[120:123]\n"
    "v_add_f32 v203, v203, v26\n"
    "v_exp_f32 v28, v28\n"
    "v_mfma_f32_16x16x32_bf16 v[124:127], v[196:199], a[248:251], v[124:127]\n"
    "s_add_u32 s67, s67, 16384\n"
    "s_add_u32 m0, s65, 8192\n"
    "ds_read_b64_tr_b16 a[30:31], v226 offset:30720\n"
    "ds_read_b64_tr_b16 a[32:33], v226 offset:32768\n"
    "ds_read_b64_tr_b16 a[34:35], v226 offset:34816\n"
    "v_mfma_f32_16x16x32_bf16 v[128:131], v[184:187], a[252:255], v[128:131]\n"
    "s_waitcnt lgkmcnt(14)\n"
    "buffer_load_dwordx4 v212, s[52:55], s67 offen offset:0 nt lds\n"
    "v_add_f32 v203, v203, v27\n"
    "v_mfma_f32_16x16x32_bf16 v[132:135], v[188:191], a[252:255], v[132:135]\n"
    "v_cvt_pk_bf16_f32 v157, v26, v27\n"
    "v_exp_f32 v29, v29\n"
    "v_mfma_f32_16x16x32_bf16 v[136:139], v[192:195], a[252:255], v[136:139]\n"
    "ds_read_b64_tr_b16 a[36:37], v226 offset:36864\n"
    "v_exp_f32 v30, v30\n"
    "v_mfma_f32_16x16x32_bf16 v[140:143], v[196:199], a[252:255], v[140:143]\n"
    "s_waitcnt lgkmcnt(14)\n"
    "ds_read_b64_tr_b16 a[38:39], v226 offset:38912\n"
    "ds_read_b64_tr_b16 a[40:41], v226 offset:40960\n"
    "v_add_f32 v203, v203, v28\n"
    "v_mfma_f32_16x16x32_bf16 v[48:51], a[128:131], a[208:211], v[48:51]\n"
    "buffer_load_dwordx4 v213, s[52:55], s67 offen offset:1024 nt lds\n"
    "ds_read_b64_tr_b16 a[144:145], v218 offset:12288\n"
    "v_mfma_f32_16x16x32_bf16 v[52:55], a[132:135], a[208:211], v[52:55]\n"
    "ds_read_b64_tr_b16 a[146:147], v218 offset:14336\n"
    "ds_read_b64_tr_b16 a[42:43], v226 offset:43008\n"
    "v_cvt_pk_bf16_f32 v158, v28, v29\n"
    "v_mfma_f32_16x16x32_bf16 v[56:59], a[136:139], a[208:211], v[56:59]\n"
    "ds_read_b64_tr_b16 a[148:149], v219 offset:12288\n"
    "v_exp_f32 v31, v31\n"
    "v_mfma_f32_16x16x32_bf16 v[60:63], a[140:143], a[208:211], v[60:63]\n"
    "s_waitcnt lgkmcnt(15)\n"
    "ds_read_b64_tr_b16 a[150:151], v219 offset:14336\n"
    "v_add_f32 v203, v203, v29\n"
    "v_add_f32 v203, v203, v30\n"
    "v_mfma_f32_16x16x32_bf16 v[64:67], a[128:131], a[212:215], v[64:67]\n"
    "buffer_load_dwordx4 v214, s[52:55], s67 offen offset:2048 nt lds\n"
    "ds_read_b64_tr_b16 a[152:153], v220 offset:12288\n"
    "v_mfma_f32_16x16x32_bf16 v[68:71], a[132:135], a[212:215], v[68:71]\n"
    "ds_read_b64_tr_b16 a[154:155], v220 offset:14336\n"
    "v_cvt_pk_bf16_f32 v159, v30, v31\n"
    "v_add_f32 v203, v203, v31\n"
    "v_mfma_f32_16x16x32_bf16 v[72:75], a[136:139], a[212:215], v[72:75]\n"
    "ds_read_b64_tr_b16 a[156:157], v221 offset:12288\n"
    "v_exp_f32 v32, v32\n"
    "v_mfma_f32_16x16x32_bf16 v[76:79], a[140:143], a[212:215], v[76:79]\n"
    "ds_read_b64_tr_b16 a[158:159], v221 offset:14336\n"
    "v_exp_f32 v33, v33\n"
    "v_mfma_f32_16x16x32_bf16 v[80:83], a[128:131], a[216:219], v[80:83]\n"
    "buffer_load_dwordx4 v215, s[52:55], s67 offen offset:3072 nt lds\n"
    "ds_read_b128 a[232:235], v223 offset:18432\n"
    "v_mfma_f32_16x16x32_bf16 v[84:87], a[132:135], a[216:219], v[84:87]\n"
    "v_exp_f32 v34, v34\n"
    "v_add_f32 v204, v204, v32\n"
    "v_mfma_f32_16x16x32_bf16 v[88:91], a[136:139], a[216:219], v[88:91]\n"
    "v_cvt_pk_bf16_f32 v160, v32, v33\n"
    "v_exp_f32 v35, v35\n"
    "v_mfma_f32_16x16x32_bf16 v[92:95], a[140:143], a[216:219], v[92:95]\n"
    "ds_read_b128 a[236:239], v223 offset:19456\n"
    "v_add_f32 v204, v204, v33\n"
    "v_add_f32 v204, v204, v34\n"
    "v_mfma_f32_16x16x32_bf16 v[96:99], a[128:131], a[220:223], v[96:99]\n"
    "ds_read_b128 a[240:243], v223 offset:20480\n"
    "v_exp_f32 v36, v36\n"
    "v_mfma_f32_16x16x32_bf16 v[100:103], a[132:135], a[220:223], v[100:103]\n"
    "ds_read_b128 a[244:247], v223 offset:21504\n"
    "ds_read_b64_tr_b16 a[44:45], v226 offset:45056\n"
    "v_add_f32 v204, v204, v35\n"
    "v_mfma_f32_16x16x32_bf16 v[104:107], a[136:139], a[220:223], v[104:107]\n"
    "v_cvt_pk_bf16_f32 v161, v34, v35\n"
    "v_exp_f32 v37, v37\n"
    "v_mfma_f32_16x16x32_bf16 v[108:111], a[140:143], a[220:223], v[108:111]\n"
    "ds_read_b128 a[248:251], v223 offset:22528\n"
    "ds_read_b64_tr_b16 a[46:47], v226 offset:47104\n"
    "ds_read_b64_tr_b16 a[48:49], v226 offset:49152\n"
    "v_mfma_f32_16x16x32_bf16 v[112:115], a[128:131], a[224:227], v[112:115]\n"
    "ds_read_b128 a[252:255], v223 offset:23552\n"
    "v_exp_f32 v38, v38\n"
    "v_mfma_f32_16x16x32_bf16 v[116:119], a[132:135], a[224:227], v[116:119]\n"
    "ds_read_b64_tr_b16 a[50:51], v226 offset:51200\n"
    "ds_read_b64_tr_b16 a[52:53], v226 offset:53248\n"
    "v_add_f32 v204, v204, v36\n"
    "v_mfma_f32_16x16x32_bf16 v[120:123], a[136:139], a[224:227], v[120:123]\n"
    "v_cvt_pk_bf16_f32 v162, v36, v37\n"
    "v_exp_f32 v39, v39\n"
    "v_mfma_f32_16x16x32_bf16 v[124:127], a[140:143], a[224:227], v[124:127]\n"
    "s_waitcnt lgkmcnt(15)\n"
    "s_add_u32 s67, s67, 16384\n"
    "s_add_u32 m0, s65, 12288\n"
    "ds_read_b64_tr_b16 a[54:55], v226 offset:55296\n"
    "ds_read_b64_tr_b16 a[56:57], v226 offset:57344\n"
    "ds_read_b64_tr_b16 a[58:59], v226 offset:59392\n"
    "v_mfma_f32_16x16x32_bf16 v[128:131], a[128:131], a[228:231], v[128:131]\n"
    "s_waitcnt lgkmcnt(14)\n"
    "buffer_load_dwordx4 v212, s[52:55], s67 offen offset:0 nt lds\n"
    "ds_read_b64_tr_b16 a[60:61], v226 offset:61440\n"
    "v_mfma_f32_16x16x32_bf16 v[132:135], a[132:135], a[228:231], v[132:135]\n"
    "ds_read_b64_tr_b16 a[62:63], v226 offset:63488\n"
    "v_add_f32 v204, v204, v37\n"
    "v_add_f32 v204, v204, v38\n"
    "v_mfma_f32_16x16x32_bf16 v[136:139], a[136:139], a[228:231], v[136:139]\n"
    "v_cvt_pk_bf16_f32 v163, v38, v39\n"
    "v_add_f32 v204, v204, v39\n"
    "v_mfma_f32_16x16x32_bf16 v[140:143], a[140:143], a[228:231], v[140:143]\n"
    "s_waitcnt lgkmcnt(15)\n"
    "v_exp_f32 v40, v40\n"
    "v_mfma_f32_16x16x32_bf16 v[48:51], a[144:147], a[232:235], v[48:51]\n"
    "buffer_load_dwordx4 v213, s[52:55], s67 offen offset:1024 nt lds\n"
    "v_mfma_f32_16x16x32_bf16 v[52:55], a[148:151], a[232:235], v[52:55]\n"
    "v_exp_f32 v41, v41\n"
    "v_mfma_f32_16x16x32_bf16 v[56:59], a[152:155], a[232:235], v[56:59]\n"
    "v_exp_f32 v42, v42\n"
    "v_add_f32 v205, v205, v40\n"
    "v_mfma_f32_16x16x32_bf16 v[60:63], a[156:159], a[232:235], v[60:63]\n"
    "s_waitcnt lgkmcnt(14)\n"
    "v_cvt_pk_bf16_f32 v164, v40, v41\n"
    "v_exp_f32 v43, v43\n"
    "v_mfma_f32_16x16x32_bf16 v[64:67], a[144:147], a[236:239], v[64:67]\n"
    "buffer_load_dwordx4 v214, s[52:55], s67 offen offset:2048 nt lds\n"
    "v_add_f32 v205, v205, v41\n"
    "v_mfma_f32_16x16x32_bf16 v[68:71], a[148:151], a[236:239], v[68:71]\n"
    "v_add_f32 v205, v205, v42\n"
    "v_exp_f32 v44, v44\n"
    "v_mfma_f32_16x16x32_bf16 v[72:75], a[152:155], a[236:239], v[72:75]\n"
    "v_mfma_f32_16x16x32_bf16 v[76:79], a[156:159], a[236:239], v[76:79]\n"
    "s_waitcnt lgkmcnt(13)\n"
    "v_add_f32 v205, v205, v43\n"
    "v_cvt_pk_bf16_f32 v165, v42, v43\n"
    "v_mfma_f32_16x16x32_bf16 v[80:83], a[144:147], a[240:243], v[80:83]\n"
    "s_waitcnt lgkmcnt(0)\n"
    "s_barrier\n"
    "buffer_load_dwordx4 v215, s[52:55], s67 offen offset:3072 nt lds\n"
    "s_add_u32 s67, s67, 16384\n"
    "v_mfma_f32_16x16x32_bf16 v[84:87], a[148:151], a[240:243], v[84:87]\n"
    "v_exp_f32 v45, v45\n"
    "v_mfma_f32_16x16x32_bf16 v[88:91], a[152:155], a[240:243], v[88:91]\n"
    "v_exp_f32 v46, v46\n"
    "v_add_f32 v205, v205, v44\n"
    "v_mfma_f32_16x16x32_bf16 v[92:95], a[156:159], a[240:243], v[92:95]\n"
    "ds_write_b128 v222, v[144:147] offset:0\n"
    "v_cvt_pk_bf16_f32 v166, v44, v45\n"
    "v_mfma_f32_16x16x32_bf16 v[96:99], a[144:147], a[244:247], v[96:99]\n"
    "ds_write_b32 v230, v200 offset:0\n"
    "v_exp_f32 v47, v47\n"
    "v_mfma_f32_16x16x32_bf16 v[100:103], a[148:151], a[244:247], v[100:103]\n"
    "ds_write_b128 v222, v[148:151] offset:1024\n"
    "v_add_f32 v205, v205, v45\n"
    "v_mfma_f32_16x16x32_bf16 v[104:107], a[152:155], a[244:247], v[104:107]\n"
    "s_waitcnt vmcnt(12)\n"
    "ds_read_b64_tr_b16 v[168:169], v218 offset:0\n"
    "v_add_f32 v205, v205, v46\n"
    "v_cvt_pk_bf16_f32 v167, v46, v47\n"
    "v_mfma_f32_16x16x32_bf16 v[108:111], a[156:159], a[244:247], v[108:111]\n"
    "ds_write_b32 v230, v201 offset:1280\n"
    "ds_read_b64_tr_b16 v[170:171], v218 offset:2048\n"
    "v_add_f32 v205, v205, v47\n"
    "v_mfma_f32_16x16x32_bf16 v[112:115], a[144:147], a[248:251], v[112:115]\n"
    "ds_read_b64_tr_b16 v[172:173], v219 offset:0\n"
    "ds_read_b64_tr_b16 v[174:175], v219 offset:2048\n"
    "ds_read_b64_tr_b16 v[176:177], v220 offset:0\n"
    "v_mfma_f32_16x16x32_bf16 v[116:119], a[148:151], a[248:251], v[116:119]\n"
    "ds_write_b128 v222, v[152:155] offset:2048\n"
    "ds_read_b64_tr_b16 v[178:179], v220 offset:2048\n"
    "v_mfma_f32_16x16x32_bf16 v[120:123], a[152:155], a[248:251], v[120:123]\n"
    "ds_read_b64_tr_b16 v[180:181], v221 offset:0\n"
    "v_mfma_f32_16x16x32_bf16 v[124:127], a[156:159], a[248:251], v[124:127]\n"
    "ds_write_b32 v230, v202 offset:2560\n"
    "ds_read_b64_tr_b16 v[182:183], v221 offset:2048\n"
    "v_mfma_f32_16x16x32_bf16 v[128:131], a[144:147], a[252:255], v[128:131]\n"
    "v_mfma_f32_16x16x32_bf16 v[132:135], a[148:151], a[252:255], v[132:135]\n"
    "ds_write_b128 v222, v[156:159] offset:3072\n"
    "v_mfma_f32_16x16x32_bf16 v[136:139], a[152:155], a[252:255], v[136:139]\n"
    "v_mfma_f32_16x16x32_bf16 v[140:143], a[156:159], a[252:255], v[140:143]\n"
    "ds_write_b32 v230, v203 offset:3840\n"
    "ds_write_b128 v222, v[160:163] offset:4096\n"
    "ds_write_b32 v230, v204 offset:5120\n"
    "ds_write_b128 v222, v[164:167] offset:5120\n"
    "ds_write_b32 v230, v205 offset:6400\n"
    "s_waitcnt lgkmcnt(0)\n"
    "s_barrier\n"
    "ds_read_b128 a[208:211], v223 offset:0\n"
    "ds_read_b128 a[212:215], v223 offset:1024\n"
    "ds_read_b128 a[216:219], v223 offset:2048\n"
    "ds_read_b128 a[220:223], v223 offset:3072\n"
    "ds_read_b128 a[224:227], v223 offset:4096\n"
    "ds_read_b128 a[228:231], v223 offset:5120\n"
    "s_waitcnt lgkmcnt(5)\n"
    "v_mfma_f32_16x16x32_bf16 v[48:51], v[168:171], a[208:211], v[48:51]\n"
    "s_waitcnt vmcnt(8)\n"
    "ds_read_b64_tr_b16 v[184:185], v218 offset:4096\n"
    "v_mfma_f32_16x16x32_bf16 v[52:55], v[172:175], a[208:211], v[52:55]\n"
    "ds_read_b64_tr_b16 v[186:187], v218 offset:6144\n"
    "ds_read_b128 v[232:235], v231 offset:0\n"
    "v_mfma_f32_16x16x32_bf16 v[56:59], v[176:179], a[208:211], v[56:59]\n"
    "s_add_u32 m0, s79, 0\n"
    "s_add_u32 s74, s78, 256\n"
    "ds_read_b64_tr_b16 v[188:189], v219 offset:4096\n"
    "ds_read_b128 v[236:239], v231 offset:1280\n"
    "v_mfma_f32_16x16x32_bf16 v[60:63], v[180:183], a[208:211], v[60:63]\n"
    "s_waitcnt lgkmcnt(9)\n"
    "buffer_load_dwordx4 v208, s[56:59], s74 offen offset:0 lds\n"
    "v_mfma_f32_16x16x32_bf16 v[64:67], v[168:171], a[212:215], v[64:67]\n"
    "ds_read_b64_tr_b16 v[190:191], v219 offset:6144\n"
    "ds_read_b128 a[232:235], v223 offset:6144\n"
    "v_mfma_f32_16x16x32_bf16 v[68:71], v[172:175], a[212:215], v[68:71]\n"
    "ds_read_b128 v[240:243], v231 offset:2560\n"
    "ds_read_b64_tr_b16 v[192:193], v220 offset:4096\n"
    "v_mfma_f32_16x16x32_bf16 v[72:75], v[176:179], a[212:215], v[72:75]\n"
    "ds_read_b128 v[244:247], v231 offset:3840\n"
    "ds_read_b64_tr_b16 v[194:195], v220 offset:6144\n"
    "v_mfma_f32_16x16x32_bf16 v[76:79], v[180:183], a[212:215], v[76:79]\n"
    "s_waitcnt lgkmcnt(14)\n"
    "ds_read_b128 v[248:251], v231 offset:5120\n"
    "ds_read_b64_tr_b16 v[196:197], v221 offset:4096\n"
    "v_mfma_f32_16x16x32_bf16 v[80:83], v[168:171], a[216:219], v[80:83]\n"
    "buffer_load_dwordx4 v209, s[56:59], s74 offen offset:1024 lds\n"
    "v_mfma_f32_16x16x32_bf16 v[84:87], v[172:175], a[216:219], v[84:87]\n"
    "s_mov_b32 s95, 0\n"
    "LCHK_%=:\n"
    "v_cmp_ne_u32 vcc, v207, v225\n"
    "s_nop 0\n"
    "s_cbranch_vccz LOK_%=\n"
    "buffer_load_dword v225, v228, s[96:99], s87 offen sc1\n"
    "s_sleep 2\n"
    "s_waitcnt vmcnt(0)\n"
    "s_add_u32 s95, s95, 1\n"
    "s_cmp_lt_u32 s95, 0x40000\n"
    "s_cbranch_scc1 LCHK_%=\n"
    "LOK_%=:\n"
    "v_lshrrev_b32 v228, 6, v228\n"
    "v_mfma_f32_16x16x32_bf16 v[88:91], v[176:179], a[216:219], v[88:91]\n"
    "ds_read_b128 v[252:255], v231 offset:6400\n"
    "ds_read_b64_tr_b16 v[198:199], v221 offset:6144\n"
    "v_mfma_f32_16x16x32_bf16 v[92:95], v[180:183], a[216:219], v[92:95]\n"
    "s_waitcnt lgkmcnt(15)\n"
    "buffer_load_dwordx4 v210, s[56:59], s74 offen offset:2048 lds\n"
    "v_mfma_f32_16x16x32_bf16 v[96:99], v[168:171], a[220:223], v[96:99]\n"
    "ds_read_b128 a[236:239], v223 offset:7168\n"
    "ds_read_b128 a[240:243], v223 offset:8192\n"
    "v_mfma_f32_16x16x32_bf16 v[100:103], v[172:175], a[220:223], v[100:103]\n"
    "ds_read_b128 a[244:247], v223 offset:9216\n"
    "ds_read_b128 a[248:251], v223 offset:10240\n"
    "v_mfma_f32_16x16x32_bf16 v[104:107], v[176:179], a[220:223], v[104:107]\n"
    "buffer_load_dwordx4 v211, s[56:59], s74 offen offset:3072 lds\n"
    "v_mfma_f32_16x16x32_bf16 v[108:111], v[180:183], a[220:223], v[108:111]\n"
    "ds_read_b128 a[252:255], v223 offset:11264\n"
    "v_add_u32 v214, s84, v224\n"
    "v_mfma_f32_16x16x32_bf16 v[112:115], v[168:171], a[224:227], v[112:115]\n"
    "v_add_u32 v215, s85, v224\n"
    "v_add_u32 v212, 65536, v226\n"
    "v_mfma_f32_16x16x32_bf16 v[116:119], v[172:175], a[224:227], v[116:119]\n"
    "s_add_u32 m0, s79, 4096\n"
    "s_add_u32 s74, s78, 28928\n"
    "v_add_u32 v213, 131072, v226\n"
    "s_waitcnt lgkmcnt(15)\n"
    "v_add_f32 v232, v232, v233\n"
    "v_mfma_f32_16x16x32_bf16 v[120:123], v[176:179], a[224:227], v[120:123]\n"
    "buffer_load_dwordx4 v208, s[56:59], s74 offen offset:0 lds\n"
    "v_mfma_f32_16x16x32_bf16 v[124:127], v[180:183], a[224:227], v[124:127]\n"
    "v_add_f32 v236, v236, v237\n"
    "s_waitcnt lgkmcnt(12)\n"
    "v_add_f32 v240, v240, v241\n"
    "v_mfma_f32_16x16x32_bf16 v[128:131], v[168:171], a[228:231], v[128:131]\n"
    "s_waitcnt lgkmcnt(10)\n"
    "v_add_f32 v244, v244, v245\n"
    "s_waitcnt lgkmcnt(8)\n"
    "v_add_f32 v248, v248, v249\n"
    "v_mfma_f32_16x16x32_bf16 v[132:135], v[172:175], a[228:231], v[132:135]\n"
    "s_waitcnt lgkmcnt(6)\n"
    "v_add_f32 v252, v252, v253\n"
    "v_add_f32 v234, v234, v235\n"
    "v_mfma_f32_16x16x32_bf16 v[136:139], v[176:179], a[228:231], v[136:139]\n"
    "buffer_load_dwordx4 v209, s[56:59], s74 offen offset:1024 lds\n"
    "v_mfma_f32_16x16x32_bf16 v[140:143], v[180:183], a[228:231], v[140:143]\n"
    "v_add_f32 v238, v238, v239\n"
    "v_add_f32 v242, v242, v243\n"
    "v_mfma_f32_16x16x32_bf16 v[48:51], v[184:187], a[232:235], v[48:51]\n"
    "v_add_f32 v246, v246, v247\n"
    "v_add_f32 v250, v250, v251\n"
    "v_mfma_f32_16x16x32_bf16 v[52:55], v[188:191], a[232:235], v[52:55]\n"
    "s_waitcnt vmcnt(10)\n"
    "ds_read_b64_tr_b16 v[168:169], v218 offset:8192\n"
    "v_add_f32 v254, v254, v255\n"
    "v_mfma_f32_16x16x32_bf16 v[56:59], v[192:195], a[232:235], v[56:59]\n"
    "s_waitcnt lgkmcnt(6)\n"
    "ds_read_b64_tr_b16 v[170:171], v218 offset:10240\n"
    "v_add_f32 v200, v232, v234\n"
    "v_mfma_f32_16x16x32_bf16 v[60:63], v[196:199], a[232:235], v[60:63]\n"
    "s_waitcnt lgkmcnt(6)\n"
    "ds_read_b64_tr_b16 v[172:173], v219 offset:8192\n"
    "v_add_f32 v201, v236, v238\n"
    "v_mfma_f32_16x16x32_bf16 v[64:67], v[184:187], a[236:239], v[64:67]\n"
    "buffer_load_dwordx4 v210, s[56:59], s74 offen offset:2048 lds\n"
    "v_mfma_f32_16x16x32_bf16 v[68:71], v[188:191], a[236:239], v[68:71]\n"
    "ds_read_b64_tr_b16 v[174:175], v219 offset:10240\n"
    "ds_read_b128 a[208:211], v223 offset:12288\n"
    "v_mfma_f32_16x16x32_bf16 v[72:75], v[192:195], a[236:239], v[72:75]\n"
    "ds_read_b64_tr_b16 v[176:177], v220 offset:8192\n"
    "v_add_f32 v202, v240, v242\n"
    "v_mfma_f32_16x16x32_bf16 v[76:79], v[196:199], a[236:239], v[76:79]\n"
    "s_waitcnt lgkmcnt(9)\n"
    "ds_read_b64_tr_b16 v[178:179], v220 offset:10240\n"
    "v_add_f32 v203, v244, v246\n"
    "v_mfma_f32_16x16x32_bf16 v[80:83], v[184:187], a[240:243], v[80:83]\n"
    "buffer_load_dwordx4 v211, s[56:59], s74 offen offset:3072 lds\n"
    "v_mfma_f32_16x16x32_bf16 v[84:87], v[188:191], a[240:243], v[84:87]\n"
    "s_add_u32 m0, s79, 32768\n"
    "s_add_u32 s74, s78, 384\n"
    "ds_read_b64_tr_b16 v[180:181], v221 offset:8192\n"
    "ds_read_b64_tr_b16 v[182:183], v221 offset:10240\n"
    "v_mfma_f32_16x16x32_bf16 v[88:91], v[192:195], a[240:243], v[88:91]\n"
    "buffer_load_dwordx4 v208, s[56:59], s74 offen offset:0 lds\n"
    "v_mfma_f32_16x16x32_bf16 v[92:95], v[196:199], a[240:243], v[92:95]\n"
    "s_waitcnt lgkmcnt(11)\n"
    "ds_read_b128 a[212:215], v223 offset:13312\n"
    "ds_read_b128 a[216:219], v223 offset:14336\n"
    "v_mfma_f32_16x16x32_bf16 v[96:99], v[184:187], a[244:247], v[96:99]\n"
    "ds_read_b128 a[220:223], v223 offset:15360\n"
    "v_add_f32 v204, v248, v250\n"
    "v_mfma_f32_16x16x32_bf16 v[100:103], v[188:191], a[244:247], v[100:103]\n"
    "ds_read_b128 a[224:227], v223 offset:16384\n"
    "v_add_f32 v205, v252, v254\n"
    "v_mfma_f32_16x16x32_bf16 v[104:107], v[192:195], a[244:247], v[104:107]\n"
    "buffer_load_dwordx4 v209, s[56:59], s74 offen offset:1024 lds\n"
    "v_mfma_f32_16x16x32_bf16 v[108:111], v[196:199], a[244:247], v[108:111]\n"
    "s_waitcnt lgkmcnt(14)\n"
    "ds_read_b128 a[228:231], v223 offset:17408\n"
    "s_waitcnt vmcnt(10)\n"
    "ds_read_b64_tr_b16 a[128:129], v218 offset:12288\n"
    "v_mfma_f32_16x16x32_bf16 v[112:115], v[184:187], a[248:251], v[112:115]\n"
    "ds_read_b64_tr_b16 a[130:131], v218 offset:14336\n"
    "ds_read_b64_tr_b16 a[132:133], v219 offset:12288\n"
    "v_mfma_f32_16x16x32_bf16 v[116:119], v[188:191], a[248:251], v[116:119]\n"
    "ds_read_b64_tr_b16 a[134:135], v219 offset:14336\n"
    "ds_read_b64_tr_b16 a[136:137], v220 offset:12288\n"
    "v_mfma_f32_16x16x32_bf16 v[120:123], v[192:195], a[248:251], v[120:123]\n"
    "ds_read_b64_tr_b16 a[138:139], v220 offset:14336\n"
    "ds_read_b64_tr_b16 a[140:141], v221 offset:12288\n"
    "v_mfma_f32_16x16x32_bf16 v[124:127], v[196:199], a[248:251], v[124:127]\n"
    "s_waitcnt lgkmcnt(15)\n"
    "ds_read_b64_tr_b16 a[142:143], v221 offset:14336\n"
    "ds_read_b128 a[144:147], v223 offset:18432\n"
    "v_mfma_f32_16x16x32_bf16 v[128:131], v[184:187], a[252:255], v[128:131]\n"
    "buffer_load_dwordx4 v210, s[56:59], s74 offen offset:2048 lds\n"
    "v_mfma_f32_16x16x32_bf16 v[132:135], v[188:191], a[252:255], v[132:135]\n"
    "ds_read_b128 a[148:151], v223 offset:19456\n"
    "v_mov_b32 v232, v200\n"
    "v_mfma_f32_16x16x32_bf16 v[136:139], v[192:195], a[252:255], v[136:139]\n"
    "buffer_load_dwordx4 v211, s[56:59], s74 offen offset:3072 lds\n"
    "v_mfma_f32_16x16x32_bf16 v[140:143], v[196:199], a[252:255], v[140:143]\n"
    "v_mov_b32 v233, v201\n"
    "v_mov_b32 v234, v202\n"
    "v_mfma_f32_16x16x32_bf16 v[48:51], v[168:171], a[208:211], v[48:51]\n"
    "v_mov_b32 v235, v203\n"
    "v_mov_b32 v236, v204\n"
    "v_mfma_f32_16x16x32_bf16 v[52:55], v[172:175], a[208:211], v[52:55]\n"
    "s_waitcnt lgkmcnt(15)\n"
    "s_add_u32 m0, s79, 36864\n"
    "s_add_u32 s74, s78, 29056\n"
    "v_mov_b32 v237, v205\n"
    "v_permlane32_swap_b32 v200, v232\n"
    "v_mfma_f32_16x16x32_bf16 v[56:59], v[176:179], a[208:211], v[56:59]\n"
    "buffer_load_dwordx4 v208, s[56:59], s74 offen offset:0 lds\n"
    "v_mfma_f32_16x16x32_bf16 v[60:63], v[180:183], a[208:211], v[60:63]\n"
    "s_waitcnt lgkmcnt(1)\n"
    "v_permlane32_swap_b32 v201, v233\n"
    "v_permlane32_swap_b32 v202, v234\n"
    "v_mfma_f32_16x16x32_bf16 v[48:51], a[128:131], a[144:147], v[48:51]\n"
    "v_permlane32_swap_b32 v203, v235\n"
    "v_permlane32_swap_b32 v204, v236\n"
    "v_mfma_f32_16x16x32_bf16 v[52:55], a[132:135], a[144:147], v[52:55]\n"
    "v_permlane32_swap_b32 v205, v237\n"
    "v_add_f32 v200, v200, v232\n"
    "v_mfma_f32_16x16x32_bf16 v[56:59], a[136:139], a[144:147], v[56:59]\n"
    "v_add_f32 v201, v201, v233\n"
    "v_add_f32 v202, v202, v234\n"
    "v_mfma_f32_16x16x32_bf16 v[60:63], a[140:143], a[144:147], v[60:63]\n"
    "v_add_f32 v203, v203, v235\n"
    "v_add_f32 v204, v204, v236\n"
    "v_mfma_f32_16x16x32_bf16 v[64:67], v[168:171], a[212:215], v[64:67]\n"
    "ds_read_b128 a[152:155], v223 offset:20480\n"
    "v_add_f32 v205, v205, v237\n"
    "v_mfma_f32_16x16x32_bf16 v[68:71], v[172:175], a[212:215], v[68:71]\n"
    "buffer_load_dwordx4 v209, s[56:59], s74 offen offset:1024 lds\n"
    "v_mfma_f32_16x16x32_bf16 v[72:75], v[176:179], a[212:215], v[72:75]\n"
    "buffer_load_dwordx4 v210, s[56:59], s74 offen offset:2048 lds\n"
    "v_mfma_f32_16x16x32_bf16 v[76:79], v[180:183], a[212:215], v[76:79]\n"
    "s_waitcnt lgkmcnt(1)\n"
    "ds_read_b128 a[156:159], v223 offset:21504\n"
    "ds_read_b128 a[160:163], v223 offset:22528\n"
    "v_mfma_f32_16x16x32_bf16 v[64:67], a[128:131], a[148:151], v[64:67]\n"
    "ds_read_b128 a[164:167], v223 offset:23552\n"
    "v_cvt_pk_bf16_f32 v144, v48, v49\n"
    "v_mfma_f32_16x16x32_bf16 v[68:71], a[132:135], a[148:151], v[68:71]\n"
    "v_cvt_pk_bf16_f32 v145, v50, v51\n"
    "v_cvt_pk_bf16_f32 v146, v52, v53\n"
    "v_mfma_f32_16x16x32_bf16 v[72:75], a[136:139], a[148:151], v[72:75]\n"
    "buffer_load_dwordx4 v211, s[56:59], s74 offen offset:3072 lds\n"
    "v_mfma_f32_16x16x32_bf16 v[76:79], a[140:143], a[148:151], v[76:79]\n"
    "v_cvt_pk_bf16_f32 v147, v54, v55\n"
    "v_mfma_f32_16x16x32_bf16 v[80:83], v[168:171], a[216:219], v[80:83]\n"
    "ds_write_b128 v214, v[144:147] offset:0\n"
    "v_mfma_f32_16x16x32_bf16 v[84:87], v[172:175], a[216:219], v[84:87]\n"
    "v_cvt_pk_bf16_f32 v0, v56, v57\n"
    "v_cvt_pk_bf16_f32 v1, v58, v59\n"
    "v_mfma_f32_16x16x32_bf16 v[88:91], v[176:179], a[216:219], v[88:91]\n"
    "v_cvt_pk_bf16_f32 v2, v60, v61\n"
    "v_cvt_pk_bf16_f32 v3, v62, v63\n"
    "v_mfma_f32_16x16x32_bf16 v[92:95], v[180:183], a[216:219], v[92:95]\n"
    "s_waitcnt lgkmcnt(4)\n"
    "ds_write_b128 v214, v[0:3] offset:6144\n"
    "v_mfma_f32_16x16x32_bf16 v[80:83], a[128:131], a[152:155], v[80:83]\n"
    "v_cvt_pk_bf16_f32 v148, v64, v65\n"
    "v_cvt_pk_bf16_f32 v149, v66, v67\n"
    "v_mfma_f32_16x16x32_bf16 v[84:87], a[132:135], a[152:155], v[84:87]\n"
    "v_cvt_pk_bf16_f32 v150, v68, v69\n"
    "v_cvt_pk_bf16_f32 v151, v70, v71\n"
    "v_mfma_f32_16x16x32_bf16 v[88:91], a[136:139], a[152:155], v[88:91]\n"
    "ds_write_b128 v214, v[148:151] offset:1024\n"
    "v_mfma_f32_16x16x32_bf16 v[92:95], a[140:143], a[152:155], v[92:95]\n"
    "v_cvt_pk_bf16_f32 v4, v72, v73\n"
    "v_cvt_pk_bf16_f32 v5, v74, v75\n"
    "v_mfma_f32_16x16x32_bf16 v[96:99], v[168:171], a[220:223], v[96:99]\n"
    "v_cvt_pk_bf16_f32 v6, v76, v77\n"
    "v_cvt_pk_bf16_f32 v7, v78, v79\n"
    "v_mfma_f32_16x16x32_bf16 v[100:103], v[172:175], a[220:223], v[100:103]\n"
    "ds_write_b128 v214, v[4:7] offset:7168\n"
    "v_mfma_f32_16x16x32_bf16 v[104:107], v[176:179], a[220:223], v[104:107]\n"
    "v_mfma_f32_16x16x32_bf16 v[108:111], v[180:183], a[220:223], v[108:111]\n"
    "s_waitcnt lgkmcnt(6)\n"
    "v_cvt_pk_bf16_f32 v152, v80, v81\n"
    "v_cvt_pk_bf16_f32 v153, v82, v83\n"
    "v_mfma_f32_16x16x32_bf16 v[96:99], a[128:131], a[156:159], v[96:99]\n"
    "v_cvt_pk_bf16_f32 v154, v84, v85\n"
    "v_cvt_pk_bf16_f32 v155, v86, v87\n"
    "v_mfma_f32_16x16x32_bf16 v[100:103], a[132:135], a[156:159], v[100:103]\n"
    "ds_write_b128 v214, v[152:155] offset:2048\n"
    "v_mfma_f32_16x16x32_bf16 v[104:107], a[136:139], a[156:159], v[104:107]\n"
    "v_cvt_pk_bf16_f32 v8, v88, v89\n"
    "v_cvt_pk_bf16_f32 v9, v90, v91\n"
    "v_mfma_f32_16x16x32_bf16 v[108:111], a[140:143], a[156:159], v[108:111]\n"
    "v_cvt_pk_bf16_f32 v10, v92, v93\n"
    "v_cvt_pk_bf16_f32 v11, v94, v95\n"
    "v_mfma_f32_16x16x32_bf16 v[112:115], v[168:171], a[224:227], v[112:115]\n"
    "ds_write_b128 v214, v[8:11] offset:8192\n"
    "v_mfma_f32_16x16x32_bf16 v[116:119], v[172:175], a[224:227], v[116:119]\n"
    "v_mfma_f32_16x16x32_bf16 v[120:123], v[176:179], a[224:227], v[120:123]\n"
    "v_mfma_f32_16x16x32_bf16 v[124:127], v[180:183], a[224:227], v[124:127]\n"
    "s_waitcnt lgkmcnt(7)\n"
    "v_cvt_pk_bf16_f32 v156, v96, v97\n"
    "v_cvt_pk_bf16_f32 v157, v98, v99\n"
    "v_mfma_f32_16x16x32_bf16 v[112:115], a[128:131], a[160:163], v[112:115]\n"
    "v_cvt_pk_bf16_f32 v158, v100, v101\n"
    "v_cvt_pk_bf16_f32 v159, v102, v103\n"
    "v_mfma_f32_16x16x32_bf16 v[116:119], a[132:135], a[160:163], v[116:119]\n"
    "ds_write_b128 v214, v[156:159] offset:3072\n"
    "v_mfma_f32_16x16x32_bf16 v[120:123], a[136:139], a[160:163], v[120:123]\n"
    "v_cvt_pk_bf16_f32 v12, v104, v105\n"
    "v_cvt_pk_bf16_f32 v13, v106, v107\n"
    "v_mfma_f32_16x16x32_bf16 v[124:127], a[140:143], a[160:163], v[124:127]\n"
    "v_cvt_pk_bf16_f32 v14, v108, v109\n"
    "v_cvt_pk_bf16_f32 v15, v110, v111\n"
    "v_mfma_f32_16x16x32_bf16 v[128:131], v[168:171], a[228:231], v[128:131]\n"
    "ds_write_b128 v214, v[12:15] offset:9216\n"
    "v_mfma_f32_16x16x32_bf16 v[132:135], v[172:175], a[228:231], v[132:135]\n"
    "v_mfma_f32_16x16x32_bf16 v[136:139], v[176:179], a[228:231], v[136:139]\n"
    "v_mfma_f32_16x16x32_bf16 v[140:143], v[180:183], a[228:231], v[140:143]\n"
    "s_waitcnt lgkmcnt(8)\n"
    "v_cvt_pk_bf16_f32 v160, v112, v113\n"
    "v_cvt_pk_bf16_f32 v161, v114, v115\n"
    "v_mfma_f32_16x16x32_bf16 v[128:131], a[128:131], a[164:167], v[128:131]\n"
    "v_cvt_pk_bf16_f32 v162, v116, v117\n"
    "v_cvt_pk_bf16_f32 v163, v118, v119\n"
    "v_mfma_f32_16x16x32_bf16 v[132:135], a[132:135], a[164:167], v[132:135]\n"
    "ds_write_b128 v214, v[160:163] offset:4096\n"
    "v_mfma_f32_16x16x32_bf16 v[136:139], a[136:139], a[164:167], v[136:139]\n"
    "v_cvt_pk_bf16_f32 v16, v120, v121\n"
    "v_cvt_pk_bf16_f32 v17, v122, v123\n"
    "v_mfma_f32_16x16x32_bf16 v[140:143], a[140:143], a[164:167], v[140:143]\n"
    "v_cvt_pk_bf16_f32 v18, v124, v125\n"
    "v_cvt_pk_bf16_f32 v19, v126, v127\n"
    "ds_write_b128 v214, v[16:19] offset:10240\n"
    "v_cvt_pk_bf16_f32 v164, v128, v129\n"
    "v_cvt_pk_bf16_f32 v165, v130, v131\n"
    "v_cvt_pk_bf16_f32 v166, v132, v133\n"
    "v_cvt_pk_bf16_f32 v167, v134, v135\n"
    "ds_write_b128 v214, v[164:167] offset:5120\n"
    "v_cvt_pk_bf16_f32 v20, v136, v137\n"
    "v_cvt_pk_bf16_f32 v21, v138, v139\n"
    "v_cvt_pk_bf16_f32 v22, v140, v141\n"
    "v_cvt_pk_bf16_f32 v23, v142, v143\n"
    "ds_write_b128 v214, v[20:23] offset:11264\n"
    "s_waitcnt vmcnt(0) lgkmcnt(0)\n"
    "s_barrier\n"
    "ds_read_b128 v[48:51], v224 offset:0\n"
    "ds_read_b128 v[52:55], v224 offset:1024\n"
    "ds_read_b128 v[56:59], v224 offset:2048\n"
    "ds_read_b128 v[60:63], v224 offset:3072\n"
    "ds_read_b128 v[64:67], v224 offset:4096\n"
    "ds_read_b128 v[68:71], v224 offset:5120\n"
    "ds_read_b128 v[72:75], v224 offset:6144\n"
    "ds_read_b128 v[76:79], v224 offset:7168\n"
    "ds_read_b128 v[80:83], v224 offset:8192\n"
    "ds_read_b128 v[84:87], v224 offset:9216\n"
    "ds_read_b128 v[88:91], v224 offset:10240\n"
    "ds_read_b128 v[92:95], v224 offset:11264\n"
    "s_waitcnt lgkmcnt(11)\n"
    "v_mfma_f32_16x16x32_bf16 v[0:3], a[0:3], v[48:51], 0\n"
    "s_waitcnt lgkmcnt(10)\n"
    "ds_read_b128 v[96:99], v224 offset:16384\n"
    "v_mfma_f32_16x16x32_bf16 v[4:7], a[0:3], v[52:55], 0\n"
    "s_waitcnt lgkmcnt(10)\n"
    "ds_read_b128 v[100:103], v224 offset:17408\n"
    "v_mfma_f32_16x16x32_bf16 v[8:11], a[0:3], v[56:59], 0\n"
    "s_waitcnt lgkmcnt(10)\n"
    "s_add_u32 m0, s79, 131072\n"
    "s_add_u32 s74, s78, 512\n"
    "ds_read_b128 v[104:107], v224 offset:18432\n"
    "v_mfma_f32_16x16x32_bf16 v[12:15], a[0:3], v[60:63], 0\n"
    "s_waitcnt lgkmcnt(10)\n"
    "buffer_load_dwordx4 v208, s[56:59], s74 offen offset:0 lds\n"
    "v_mfma_f32_16x16x32_bf16 v[16:19], a[0:3], v[64:67], 0\n"
    "s_waitcnt lgkmcnt(9)\n"
    "ds_read_b128 v[108:111], v224 offset:19456\n"
    "ds_read_b128 v[112:115], v224 offset:20480\n"
    "v_mfma_f32_16x16x32_bf16 v[20:23], a[0:3], v[68:71], 0\n"
    "s_waitcnt lgkmcnt(10)\n"
    "ds_read_b128 v[116:119], v224 offset:21504\n"
    "v_mfma_f32_16x16x32_bf16 v[0:3], a[4:7], v[72:75], v[0:3]\n"
    "s_waitcnt lgkmcnt(10)\n"
    "ds_read_b128 v[120:123], v224 offset:22528\n"
    "v_mov_b32 v232, v200\n"
    "v_mfma_f32_16x16x32_bf16 v[4:7], a[4:7], v[76:79], v[4:7]\n"
    "s_waitcnt lgkmcnt(10)\n"
    "buffer_load_dwordx4 v209, s[56:59], s74 offen offset:1024 lds\n"
    "v_mfma_f32_16x16x32_bf16 v[8:11], a[4:7], v[80:83], v[8:11]\n"
    "s_waitcnt lgkmcnt(9)\n"
    "ds_read_b128 v[124:127], v224 offset:23552\n"
    "ds_read_b128 v[128:131], v224 offset:24576\n"
    "v_mfma_f32_16x16x32_bf16 v[12:15], a[4:7], v[84:87], v[12:15]\n"
    "s_waitcnt lgkmcnt(10)\n"
    "ds_read_b128 v[132:135], v224 offset:25600\n"
    "v_mov_b32 v233, v201\n"
    "v_mfma_f32_16x16x32_bf16 v[16:19], a[4:7], v[88:91], v[16:19]\n"
    "s_waitcnt lgkmcnt(10)\n"
    "ds_read_b128 v[136:139], v224 offset:26624\n"
    "v_mov_b32 v234, v202\n"
    "v_mfma_f32_16x16x32_bf16 v[20:23], a[4:7], v[92:95], v[20:23]\n"
    "s_waitcnt lgkmcnt(10)\n"
    "buffer_load_dwordx4 v210, s[56:59], s74 offen offset:2048 lds\n"
    "v_mfma_f32_16x16x32_bf16 v[0:3], a[8:11], v[96:99], v[0:3]\n"
    "s_waitcnt lgkmcnt(9)\n"
    "ds_read_b128 v[140:143], v224 offset:27648\n"
    "ds_read_b128 a[192:195], v224 offset:32768\n"
    "v_mfma_f32_16x16x32_bf16 v[4:7], a[8:11], v[100:103], v[4:7]\n"
    "s_waitcnt lgkmcnt(10)\n"
    "ds_read_b128 a[196:199], v224 offset:33792\n"
    "v_mov_b32 v235, v203\n"
    "v_mfma_f32_16x16x32_bf16 v[8:11], a[8:11], v[104:107], v[8:11]\n"
    "s_waitcnt lgkmcnt(10)\n"
    "ds_read_b128 a[200:203], v224 offset:34816\n"
    "v_mov_b32 v236, v204\n"
    "v_mfma_f32_16x16x32_bf16 v[12:15], a[8:11], v[108:111], v[12:15]\n"
    "s_waitcnt lgkmcnt(10)\n"
    "buffer_load_dwordx4 v211, s[56:59], s74 offen offset:3072 lds\n"
    "v_mfma_f32_16x16x32_bf16 v[16:19], a[8:11], v[112:115], v[16:19]\n"
    "s_waitcnt lgkmcnt(9)\n"
    "ds_read_b128 a[204:207], v224 offset:35840\n"
    "ds_read_b128 a[208:211], v224 offset:36864\n"
    "v_mfma_f32_16x16x32_bf16 v[20:23], a[8:11], v[116:119], v[20:23]\n"
    "s_waitcnt lgkmcnt(10)\n"
    "ds_read_b128 a[212:215], v224 offset:37888\n"
    "v_mov_b32 v237, v205\n"
    "v_mfma_f32_16x16x32_bf16 v[0:3], a[12:15], v[120:123], v[0:3]\n"
    "s_waitcnt lgkmcnt(10)\n"
    "s_add_u32 m0, s79, 135168\n"
    "s_add_u32 s74, s78, 29184\n"
    "ds_read_b128 a[216:219], v224 offset:38912\n"
    "v_permlane16_swap_b32 v200, v232\n"
    "v_mfma_f32_16x16x32_bf16 v[4:7], a[12:15], v[124:127], v[4:7]\n"
    "s_waitcnt lgkmcnt(10)\n"
    "buffer_load_dwordx4 v208, s[56:59], s74 offen offset:0 lds\n"
    "v_mfma_f32_16x16x32_bf16 v[8:11], a[12:15], v[128:131], v[8:11]\n"
    "s_waitcnt lgkmcnt(9)\n"
    "ds_read_b128 a[220:223], v224 offset:39936\n"
    "ds_read_b128 a[224:227], v224 offset:40960\n"
    "v_mfma_f32_16x16x32_bf16 v[12:15], a[12:15], v[132:135], v[12:15]\n"
    "s_waitcnt lgkmcnt(10)\n"
    "ds_read_b128 a[228:231], v224 offset:41984\n"
    "v_permlane16_swap_b32 v201, v233\n"
    "v_mfma_f32_16x16x32_bf16 v[16:19], a[12:15], v[136:139], v[16:19]\n"
    "s_waitcnt lgkmcnt(10)\n"
    "ds_read_b128 a[232:235], v224 offset:43008\n"
    "v_permlane16_swap_b32 v202, v234\n"
    "v_mfma_f32_16x16x32_bf16 v[20:23], a[12:15], v[140:143], v[20:23]\n"
    "s_waitcnt lgkmcnt(10)\n"
    "buffer_load_dwordx4 v209, s[56:59], s74 offen offset:1024 lds\n"
    "v_mfma_f32_16x16x32_bf16 v[0:3], a[16:19], a[192:195], v[0:3]\n"
    "s_waitcnt lgkmcnt(9)\n"
    "ds_read_b128 a[236:239], v224 offset:44032\n"
    "ds_read_b128 a[240:243], v224 offset:49152\n"
    "v_mfma_f32_16x16x32_bf16 v[4:7], a[16:19], a[196:199], v[4:7]\n"
    "s_waitcnt lgkmcnt(10)\n"
    "ds_read_b128 a[244:247], v224 offset:50176\n"
    "v_permlane16_swap_b32 v203, v235\n"
    "v_mfma_f32_16x16x32_bf16 v[8:11], a[16:19], a[200:203], v[8:11]\n"
    "s_waitcnt lgkmcnt(10)\n"
    "ds_read_b128 a[248:251], v224 offset:51200\n"
    "v_permlane16_swap_b32 v204, v236\n"
    "v_mfma_f32_16x16x32_bf16 v[12:15], a[16:19], a[204:207], v[12:15]\n"
    "s_waitcnt lgkmcnt(10)\n"
    "buffer_load_dwordx4 v210, s[56:59], s74 offen offset:2048 lds\n"
    "v_mfma_f32_16x16x32_bf16 v[16:19], a[16:19], a[208:211], v[16:19]\n"
    "s_waitcnt lgkmcnt(9)\n"
    "ds_read_b128 a[252:255], v224 offset:52224\n"
    "ds_read_b128 v[168:171], v224 offset:53248\n"
    "v_mfma_f32_16x16x32_bf16 v[20:23], a[16:19], a[212:215], v[20:23]\n"
    "s_waitcnt lgkmcnt(10)\n"
    "ds_read_b128 v[172:175], v224 offset:54272\n"
    "v_permlane16_swap_b32 v205, v237\n"
    "v_mfma_f32_16x16x32_bf16 v[0:3], a[20:23], a[216:219], v[0:3]\n"
    "s_waitcnt lgkmcnt(10)\n"
    "ds_read_b128 v[176:179], v224 offset:55296\n"
    "v_add_f32 v200, v200, v232\n"
    "v_mfma_f32_16x16x32_bf16 v[4:7], a[20:23], a[220:223], v[4:7]\n"
    "s_waitcnt lgkmcnt(10)\n"
    "buffer_load_dwordx4 v211, s[56:59], s74 offen offset:3072 lds\n"
    "v_mfma_f32_16x16x32_bf16 v[8:11], a[20:23], a[224:227], v[8:11]\n"
    "s_waitcnt lgkmcnt(9)\n"
    "ds_read_b128 v[180:183], v224 offset:56320\n"
    "ds_read_b128 v[184:187], v224 offset:57344\n"
    "v_mfma_f32_16x16x32_bf16 v[12:15], a[20:23], a[228:231], v[12:15]\n"
    "s_waitcnt lgkmcnt(10)\n"
    "ds_read_b128 v[188:191], v224 offset:58368\n"
    "v_add_f32 v201, v201, v233\n"
    "v_mfma_f32_16x16x32_bf16 v[16:19], a[20:23], a[232:235], v[16:19]\n"
    "s_waitcnt lgkmcnt(10)\n"
    "ds_read_b128 v[192:195], v224 offset:59392\n"
    "v_add_f32 v202, v202, v234\n"
    "v_mfma_f32_16x16x32_bf16 v[20:23], a[20:23], a[236:239], v[20:23]\n"
    "s_waitcnt lgkmcnt(10)\n"
    "ds_read_b128 v[196:199], v224 offset:60416\n"
    "v_add_f32 v203, v203, v235\n"
    "v_mfma_f32_16x16x32_bf16 v[0:3], a[24:27], a[240:243], v[0:3]\n"
    "s_waitcnt lgkmcnt(10)\n"
    "v_add_f32 v204, v204, v236\n"
    "v_add_f32 v205, v205, v237\n"
    "v_mfma_f32_16x16x32_bf16 v[4:7], a[24:27], a[244:247], v[4:7]\n"
    "s_waitcnt lgkmcnt(9)\n"
    "v_cmp_nge_f32 vcc, s77, v200\n"
    "s_or_b64 s[72:73], s[72:73], vcc\n"
    "v_cmp_nge_f32 vcc, s77, v201\n"
    "s_or_b64 s[72:73], s[72:73], vcc\n"
    "v_mfma_f32_16x16x32_bf16 v[8:11], a[24:27], a[248:251], v[8:11]\n"
    "s_waitcnt lgkmcnt(8)\n"
    "v_cmp_nge_f32 vcc, s77, v202\n"
    "s_or_b64 s[72:73], s[72:73], vcc\n"
    "v_cmp_nge_f32 vcc, s77, v203\n"
    "s_or_b64 s[72:73], s[72:73], vcc\n"
    "v_mfma_f32_16x16x32_bf16 v[12:15], a[24:27], a[252:255], v[12:15]\n"
    "s_waitcnt lgkmcnt(7)\n"
    "v_cmp_nge_f32 vcc, s77, v204\n"
    "s_or_b64 s[72:73], s[72:73], vcc\n"
    "v_cmp_nge_f32 vcc, s77, v205\n"
    "s_or_b64 s[72:73], s[72:73], vcc\n"
    "v_mfma_f32_16x16x32_bf16 v[16:19], a[24:27], v[168:171], v[16:19]\n"
    "s_waitcnt lgkmcnt(6)\n"
    "v_rcp_f32 v200, v200\n"
    "v_mfma_f32_16x16x32_bf16 v[20:23], a[24:27], v[172:175], v[20:23]\n"
    "s_waitcnt lgkmcnt(5)\n"
    "v_mfma_f32_16x16x32_bf16 v[0:3], a[28:31], v[176:179], v[0:3]\n"
    "s_waitcnt lgkmcnt(4)\n"
    "s_waitcnt lgkmcnt(0)\n"
    "s_barrier\n"
    "s_add_u32 m0, s79, 65536\n"
    "s_add_u32 s74, s78, 640\n"
    "v_rcp_f32 v201, v201\n"
    "v_mfma_f32_16x16x32_bf16 v[4:7], a[28:31], v[180:183], v[4:7]\n"
    "buffer_load_dwordx4 v208, s[56:59], s74 offen offset:0 lds\n"
    "v_mfma_f32_16x16x32_bf16 v[8:11], a[28:31], v[184:187], v[8:11]\n"
    "v_rcp_f32 v202, v202\n"
    "v_mfma_f32_16x16x32_bf16 v[12:15], a[28:31], v[188:191], v[12:15]\n"
    "v_mfma_f32_16x16x32_bf16 v[16:19], a[28:31], v[192:195], v[16:19]\n"
    "v_mfma_f32_16x16x32_bf16 v[20:23], a[28:31], v[196:199], v[20:23]\n"
    "buffer_load_dwordx4 v209, s[56:59], s74 offen offset:1024 lds\n"
    "v_mfma_f32_16x16x32_bf16 v[24:27], a[32:35], v[48:51], 0\n"
    "v_rcp_f32 v203, v203\n"
    "v_mfma_f32_16x16x32_bf16 v[28:31], a[32:35], v[52:55], 0\n"
    "v_mfma_f32_16x16x32_bf16 v[32:35], a[32:35], v[56:59], 0\n"
    "v_rcp_f32 v204, v204\n"
    "v_mfma_f32_16x16x32_bf16 v[36:39], a[32:35], v[60:63], 0\n"
    "buffer_load_dwordx4 v210, s[56:59], s74 offen offset:2048 lds\n"
    "v_mfma_f32_16x16x32_bf16 v[40:43], a[32:35], v[64:67], 0\n"
    "v_mfma_f32_16x16x32_bf16 v[44:47], a[32:35], v[68:71], 0\n"
    "v_rcp_f32 v205, v205\n"
    "v_mfma_f32_16x16x32_bf16 v[24:27], a[36:39], v[72:75], v[24:27]\n"
    "v_mul_f32 v0, v200, v0\n"
    "v_mul_f32 v1, v200, v1\n"
    "v_mfma_f32_16x16x32_bf16 v[28:31], a[36:39], v[76:79], v[28:31]\n"
    "buffer_load_dwordx4 v211, s[56:59], s74 offen offset:3072 lds\n"
    "v_mfma_f32_16x16x32_bf16 v[32:35], a[36:39], v[80:83], v[32:35]\n"
    "ds_read_b64_tr_b16 a[64:65], v226 offset:0\n"
    "v_mul_f32 v2, v200, v2\n"
    "v_mfma_f32_16x16x32_bf16 v[36:39], a[36:39], v[84:87], v[36:39]\n"
    "v_mul_f32 v3, v200, v3\n"
    "v_cvt_pk_bf16_f32 v144, v0, v1\n"
    "v_mfma_f32_16x16x32_bf16 v[40:43], a[36:39], v[88:91], v[40:43]\n"
    "s_add_u32 m0, s79, 69632\n"
    "s_add_u32 s74, s78, 29312\n"
    "ds_read_b64_tr_b16 a[66:67], v226 offset:2048\n"
    "v_cvt_pk_bf16_f32 v145, v2, v3\n"
    "v_mfma_f32_16x16x32_bf16 v[44:47], a[36:39], v[92:95], v[44:47]\n"
    "buffer_load_dwordx4 v208, s[56:59], s74 offen offset:0 lds\n"
    "v_mfma_f32_16x16x32_bf16 v[24:27], a[40:43], v[96:99], v[24:27]\n"
    "v_mul_f32 v4, v201, v4\n"
    "v_mul_f32 v5, v201, v5\n"
    "v_mfma_f32_16x16x32_bf16 v[28:31], a[40:43], v[100:103], v[28:31]\n"
    "v_mul_f32 v6, v201, v6\n"
    "v_mul_f32 v7, v201, v7\n"
    "v_mfma_f32_16x16x32_bf16 v[32:35], a[40:43], v[104:107], v[32:35]\n"
    "ds_read_b64_tr_b16 a[68:69], v226 offset:4096\n"
    "v_cvt_pk_bf16_f32 v146, v4, v5\n"
    "v_mfma_f32_16x16x32_bf16 v[36:39], a[40:43], v[108:111], v[36:39]\n"
    "buffer_load_dwordx4 v209, s[56:59], s74 offen offset:1024 lds\n"
    "v_mfma_f32_16x16x32_bf16 v[40:43], a[40:43], v[112:115], v[40:43]\n"
    "v_cvt_pk_bf16_f32 v147, v6, v7\n"
    "v_mul_f32 v8, v202, v8\n"
    "v_mfma_f32_16x16x32_bf16 v[44:47], a[40:43], v[116:119], v[44:47]\n"
    "v_mul_f32 v9, v202, v9\n"
    "v_mul_f32 v10, v202, v10\n"
    "v_mfma_f32_16x16x32_bf16 v[24:27], a[44:47], v[120:123], v[24:27]\n"
    "ds_read_b64_tr_b16 a[70:71], v226 offset:6144\n"
    "v_mul_f32 v11, v202, v11\n"
    "v_mfma_f32_16x16x32_bf16 v[28:31], a[44:47], v[124:127], v[28:31]\n"
    "s_mov_b32 s75, 0\n"
    "buffer_load_dwordx4 v210, s[56:59], s74 offen offset:2048 lds\n"
    "v_mfma_f32_16x16x32_bf16 v[32:35], a[44:47], v[128:131], v[32:35]\n"
    "buffer_store_dwordx4 v[144:147], v215, s[60:63], s75 offen offset:0 sc1\n"
    "v_mfma_f32_16x16x32_bf16 v[36:39], a[44:47], v[132:135], v[36:39]\n"
    "v_cvt_pk_bf16_f32 v148, v8, v9\n"
    "v_cvt_pk_bf16_f32 v149, v10, v11\n"
    "v_mfma_f32_16x16x32_bf16 v[40:43], a[44:47], v[136:139], v[40:43]\n"
    "v_mul_f32 v12, v203, v12\n"
    "v_mul_f32 v13, v203, v13\n"
    "v_mfma_f32_16x16x32_bf16 v[44:47], a[44:47], v[140:143], v[44:47]\n"
    "buffer_load_dwordx4 v211, s[56:59], s74 offen offset:3072 lds\n"
    "v_mfma_f32_16x16x32_bf16 v[24:27], a[48:51], a[192:195], v[24:27]\n"
    "v_mul_f32 v14, v203, v14\n"
    "v_mul_f32 v15, v203, v15\n"
    "v_mfma_f32_16x16x32_bf16 v[28:31], a[48:51], a[196:199], v[28:31]\n"
    "v_cvt_pk_bf16_f32 v150, v12, v13\n"
    "v_cvt_pk_bf16_f32 v151, v14, v15\n"
    "v_mfma_f32_16x16x32_bf16 v[32:35], a[48:51], a[200:203], v[32:35]\n"
    "s_add_u32 m0, s79, 98304\n"
    "s_add_u32 s74, s78, 768\n"
    "v_mul_f32 v16, v204, v16\n"
    "v_mul_f32 v17, v204, v17\n"
    "v_mfma_f32_16x16x32_bf16 v[36:39], a[48:51], a[204:207], v[36:39]\n"
    "buffer_load_dwordx4 v208, s[56:59], s74 offen offset:0 lds\n"
    "v_mfma_f32_16x16x32_bf16 v[40:43], a[48:51], a[208:211], v[40:43]\n"
    "v_mul_f32 v18, v204, v18\n"
    "v_mul_f32 v19, v204, v19\n"
    "v_mfma_f32_16x16x32_bf16 v[44:47], a[48:51], a[212:215], v[44:47]\n"
    "ds_read_b64_tr_b16 a[72:73], v226 offset:8192\n"
    "v_cvt_pk_bf16_f32 v152, v16, v17\n"
    "v_mfma_f32_16x16x32_bf16 v[24:27], a[52:55], a[216:219], v[24:27]\n"
    "ds_read_b64_tr_b16 a[74:75], v226 offset:10240\n"
    "v_cvt_pk_bf16_f32 v153, v18, v19\n"
    "v_mfma_f32_16x16x32_bf16 v[28:31], a[52:55], a[220:223], v[28:31]\n"
    "s_mov_b32 s75, 0\n"
    "buffer_load_dwordx4 v209, s[56:59], s74 offen offset:1024 lds\n"
    "v_mfma_f32_16x16x32_bf16 v[32:35], a[52:55], a[224:227], v[32:35]\n"
    "buffer_store_dwordx4 v[148:151], v215, s[60:63], s75 offen offset:1024 sc1\n"
    "v_mfma_f32_16x16x32_bf16 v[36:39], a[52:55], a[228:231], v[36:39]\n"
    "v_mul_f32 v20, v205, v20\n"
    "v_mul_f32 v21, v205, v21\n"
    "v_mfma_f32_16x16x32_bf16 v[40:43], a[52:55], a[232:235], v[40:43]\n"
    "v_mul_f32 v22, v205, v22\n"
    "v_mul_f32 v23, v205, v23\n"
    "v_mfma_f32_16x16x32_bf16 v[44:47], a[52:55], a[236:239], v[44:47]\n"
    "buffer_load_dwordx4 v210, s[56:59], s74 offen offset:2048 lds\n"
    "v_mfma_f32_16x16x32_bf16 v[24:27], a[56:59], a[240:243], v[24:27]\n"
    "v_cvt_pk_bf16_f32 v154, v20, v21\n"
    "v_cvt_pk_bf16_f32 v155, v22, v23\n"
    "v_mfma_f32_16x16x32_bf16 v[28:31], a[56:59], a[244:247], v[28:31]\n"
    "ds_read_b64_tr_b16 a[76:77], v226 offset:12288\n"
    "ds_read_b64_tr_b16 a[78:79], v226 offset:14336\n"
    "v_mfma_f32_16x16x32_bf16 v[32:35], a[56:59], a[248:251], v[32:35]\n"
    "ds_read_b64_tr_b16 a[80:81], v226 offset:16384\n"
    "ds_read_b64_tr_b16 a[82:83], v226 offset:18432\n"
    "v_mfma_f32_16x16x32_bf16 v[36:39], a[56:59], a[252:255], v[36:39]\n"
    "buffer_load_dwordx4 v211, s[56:59], s74 offen offset:3072 lds\n"
    "v_mfma_f32_16x16x32_bf16 v[40:43], a[56:59], v[168:171], v[40:43]\n"
    "ds_read_b64_tr_b16 a[84:85], v226 offset:20480\n"
    "ds_read_b64_tr_b16 a[86:87], v226 offset:22528\n"
    "v_mfma_f32_16x16x32_bf16 v[44:47], a[56:59], v[172:175], v[44:47]\n"
    "ds_read_b64_tr_b16 a[88:89], v226 offset:24576\n"
    "ds_read_b64_tr_b16 a[90:91], v226 offset:26624\n"
    "v_mfma_f32_16x16x32_bf16 v[24:27], a[60:63], v[176:179], v[24:27]\n"
    "s_add_u32 m0, s79, 102400\n"
    "s_add_u32 s74, s78, 29440\n"
    "ds_read_b64_tr_b16 a[92:93], v226 offset:28672\n"
    "ds_read_b64_tr_b16 a[94:95], v226 offset:30720\n"
    "v_mfma_f32_16x16x32_bf16 v[28:31], a[60:63], v[180:183], v[28:31]\n"
    "buffer_load_dwordx4 v208, s[56:59], s74 offen offset:0 lds\n"
    "v_mfma_f32_16x16x32_bf16 v[32:35], a[60:63], v[184:187], v[32:35]\n"
    "v_mfma_f32_16x16x32_bf16 v[36:39], a[60:63], v[188:191], v[36:39]\n"
    "v_mfma_f32_16x16x32_bf16 v[40:43], a[60:63], v[192:195], v[40:43]\n"
    "v_mfma_f32_16x16x32_bf16 v[44:47], a[60:63], v[196:199], v[44:47]\n"
    "s_waitcnt lgkmcnt(14)\n"
    "s_mov_b32 s75, 0\n"
    "buffer_load_dwordx4 v209, s[56:59], s74 offen offset:1024 lds\n"
    "v_mfma_f32_16x16x32_bf16 v[0:3], a[64:67], v[48:51], 0\n"
    "buffer_store_dwordx4 v[152:155], v215, s[60:63], s75 offen offset:2048 sc1\n"
    "v_mfma_f32_16x16x32_bf16 v[4:7], a[64:67], v[52:55], 0\n"
    "v_mfma_f32_16x16x32_bf16 v[8:11], a[64:67], v[56:59], 0\n"
    "v_mfma_f32_16x16x32_bf16 v[12:15], a[64:67], v[60:63], 0\n"
    "buffer_load_dwordx4 v210, s[56:59], s74 offen offset:2048 lds\n"
    "v_mfma_f32_16x16x32_bf16 v[16:19], a[64:67], v[64:67], 0\n"
    "v_mfma_f32_16x16x32_bf16 v[20:23], a[64:67], v[68:71], 0\n"
    "s_waitcnt lgkmcnt(12)\n"
    "v_mfma_f32_16x16x32_bf16 v[0:3], a[68:71], v[72:75], v[0:3]\n"
    "v_mul_f32 v24, v200, v24\n"
    "v_mul_f32 v25, v200, v25\n"
    "v_mfma_f32_16x16x32_bf16 v[4:7], a[68:71], v[76:79], v[4:7]\n"
    "buffer_load_dwordx4 v211, s[56:59], s74 offen offset:3072 lds\n"
    "v_mfma_f32_16x16x32_bf16 v[8:11], a[68:71], v[80:83], v[8:11]\n"
    "ds_read_b64_tr_b16 a[96:97], v226 offset:32768\n"
    "v_mul_f32 v26, v200, v26\n"
    "v_mfma_f32_16x16x32_bf16 v[12:15], a[68:71], v[84:87], v[12:15]\n"
    "v_mul_f32 v27, v200, v27\n"
    "v_cvt_pk_bf16_f32 v156, v24, v25\n"
    "v_mfma_f32_16x16x32_bf16 v[16:19], a[68:71], v[88:91], v[16:19]\n"
    "ds_read_b64_tr_b16 a[98:99], v226 offset:34816\n"
    "v_cvt_pk_bf16_f32 v157, v26, v27\n"
    "v_mfma_f32_16x16x32_bf16 v[20:23], a[68:71], v[92:95], v[20:23]\n"
    "s_waitcnt lgkmcnt(12)\n"
    "v_mul_f32 v28, v201, v28\n"
    "v_mul_f32 v29, v201, v29\n"
    "v_mfma_f32_16x16x32_bf16 v[0:3], a[72:75], v[96:99], v[0:3]\n"
    "ds_read_b64_tr_b16 a[100:101], v226 offset:36864\n"
    "v_mul_f32 v30, v201, v30\n"
    "v_mfma_f32_16x16x32_bf16 v[4:7], a[72:75], v[100:103], v[4:7]\n"
    "v_mul_f32 v31, v201, v31\n"
    "v_mfma_f32_16x16x32_bf16 v[8:11], a[72:75], v[104:107], v[8:11]\n"
    "ds_read_b64_tr_b16 a[102:103], v226 offset:38912\n"
    "v_cvt_pk_bf16_f32 v158, v28, v29\n"
    "v_mfma_f32_16x16x32_bf16 v[12:15], a[72:75], v[108:111], v[12:15]\n"
    "v_cvt_pk_bf16_f32 v159, v30, v31\n"
    "v_mfma_f32_16x16x32_bf16 v[16:19], a[72:75], v[112:115], v[16:19]\n"
    "v_mul_f32 v32, v202, v32\n"
    "v_mul_f32 v33, v202, v33\n"
    "v_mfma_f32_16x16x32_bf16 v[20:23], a[72:75], v[116:119], v[20:23]\n"
    "s_waitcnt lgkmcnt(12)\n"
    "ds_read_b64_tr_b16 a[104:105], v226 offset:40960\n"
    "v_mul_f32 v34, v202, v34\n"
    "v_mfma_f32_16x16x32_bf16 v[0:3], a[76:79], v[120:123], v[0:3]\n"
    "ds_read_b64_tr_b16 a[106:107], v226 offset:43008\n"
    "v_mul_f32 v35, v202, v35\n"
    "v_mfma_f32_16x16x32_bf16 v[4:7], a[76:79], v[124:127], v[4:7]\n"
    "s_mov_b32 s75, 12288\n"
    "v_cvt_pk_bf16_f32 v160, v32, v33\n"
    "v_mfma_f32_16x16x32_bf16 v[8:11], a[76:79], v[128:131], v[8:11]\n"
    "buffer_store_dwordx4 v[156:159], v215, s[60:63], s75 offen offset:0 sc1\n"
    "v_mfma_f32_16x16x32_bf16 v[12:15], a[76:79], v[132:135], v[12:15]\n"
    "v_cvt_pk_bf16_f32 v161, v34, v35\n"
    "v_mul_f32 v36, v203, v36\n"
    "v_mfma_f32_16x16x32_bf16 v[16:19], a[76:79], v[136:139], v[16:19]\n"
    "v_mul_f32 v37, v203, v37\n"
    "v_mul_f32 v38, v203, v38\n"
    "v_mfma_f32_16x16x32_bf16 v[20:23], a[76:79], v[140:143], v[20:23]\n"
    "s_waitcnt lgkmcnt(12)\n"
    "ds_read_b64_tr_b16 a[108:109], v226 offset:45056\n"
    "v_mul_f32 v39, v203, v39\n"
    "v_mfma_f32_16x16x32_bf16 v[0:3], a[80:83], a[192:195], v[0:3]\n"
    "ds_read_b64_tr_b16 a[110:111], v226 offset:47104\n"
    "v_cvt_pk_bf16_f32 v162, v36, v37\n"
    "v_mfma_f32_16x16x32_bf16 v[4:7], a[80:83], a[196:199], v[4:7]\n"
    "ds_read_b64_tr_b16 a[112:113], v226 offset:49152\n"
    "v_cvt_pk_bf16_f32 v163, v38, v39\n"
    "v_mfma_f32_16x16x32_bf16 v[8:11], a[80:83], a[200:203], v[8:11]\n"
    "v_mul_f32 v40, v204, v40\n"
    "v_mul_f32 v41, v204, v41\n"
    "v_mfma_f32_16x16x32_bf16 v[12:15], a[80:83], a[204:207], v[12:15]\n"
    "ds_read_b64_tr_b16 a[114:115], v226 offset:51200\n"
    "v_mul_f32 v42, v204, v42\n"
    "v_mfma_f32_16x16x32_bf16 v[16:19], a[80:83], a[208:211], v[16:19]\n"
    "ds_read_b64_tr_b16 a[116:117], v226 offset:53248\n"
    "v_mul_f32 v43, v204, v43\n"
    "v_mfma_f32_16x16x32_bf16 v[20:23], a[80:83], a[212:215], v[20:23]\n"
    "s_waitcnt lgkmcnt(15)\n"
    "v_cvt_pk_bf16_f32 v164, v40, v41\n"
    "v_mbcnt_lo_u32_b32 v216, -1, 0\n"
    "v_mfma_f32_16x16x32_bf16 v[0:3], a[84:87], a[216:219], v[0:3]\n"
    "ds_read_b64_tr_b16 a[118:119], v226 offset:55296\n"
    "v_cvt_pk_bf16_f32 v165, v42, v43\n"
    "v_mfma_f32_16x16x32_bf16 v[4:7], a[84:87], a[220:223], v[4:7]\n"
    "s_mov_b32 s75, 12288\n"
    "v_mul_f32 v44, v205, v44\n"
    "v_mul_f32 v45, v205, v45\n"
    "v_mfma_f32_16x16x32_bf16 v[8:11], a[84:87], a[224:227], v[8:11]\n"
    "buffer_store_dwordx4 v[160:163], v215, s[60:63], s75 offen offset:1024 sc1\n"
    "v_mfma_f32_16x16x32_bf16 v[12:15], a[84:87], a[228:231], v[12:15]\n"
    "v_mul_f32 v46, v205, v46\n"
    "v_mul_f32 v47, v205, v47\n"
    "v_mfma_f32_16x16x32_bf16 v[16:19], a[84:87], a[232:235], v[16:19]\n"
    "ds_read_b64_tr_b16 a[120:121], v226 offset:57344\n"
    "v_cvt_pk_bf16_f32 v166, v44, v45\n"
    "v_mfma_f32_16x16x32_bf16 v[20:23], a[84:87], a[236:239], v[20:23]\n"
    "s_waitcnt lgkmcnt(15)\n"
    "ds_read_b64_tr_b16 a[122:123], v226 offset:59392\n"
    "v_cvt_pk_bf16_f32 v167, v46, v47\n"
    "v_mfma_f32_16x16x32_bf16 v[0:3], a[88:91], a[240:243], v[0:3]\n"
    "ds_read_b64_tr_b16 a[124:125], v226 offset:61440\n"
    "v_mbcnt_hi_u32_b32 v216, -1, v216\n"
    "v_mfma_f32_16x16x32_bf16 v[4:7], a[88:91], a[244:247], v[4:7]\n"
    "v_mov_b32 v222, 0x7ffffff0\n"
    "s_lshr_b32 s88, s83, 7\n"
    "s_lshl2_add_u32 s88, s86, s88\n"
    "s_mul_i32 s74, s88, 24\n"
    "s_mul_i32 s95, s88, 60\n"
    "v_mfma_f32_16x16x32_bf16 v[8:11], a[88:91], a[248:251], v[8:11]\n"
    "ds_read_b64_tr_b16 a[126:127], v226 offset:63488\n"
    "v_add_u32 v217, s95, v216\n"
    "v_mfma_f32_16x16x32_bf16 v[12:15], a[88:91], a[252:255], v[12:15]\n"
    "v_mfma_f32_16x16x32_bf16 v[16:19], a[88:91], v[168:171], v[16:19]\n"
    "v_cmp_gt_u32 vcc, 60, v216\n"
    "v_mfma_f32_16x16x32_bf16 v[20:23], a[88:91], v[172:175], v[20:23]\n"
    "s_waitcnt lgkmcnt(15)\n"
    "v_mfma_f32_16x16x32_bf16 v[0:3], a[92:95], v[176:179], v[0:3]\n"
    "v_lshlrev_b32 v221, 4, v217\n"
    "v_mfma_f32_16x16x32_bf16 v[4:7], a[92:95], v[180:183], v[4:7]\n"
    "v_mfma_f32_16x16x32_bf16 v[8:11], a[92:95], v[184:187], v[8:11]\n"
    "s_waitcnt vmcnt(21)\n"
    "s_barrier\n"
    "v_mfma_f32_16x16x32_bf16 v[12:15], a[92:95], v[188:191], v[12:15]\n"
    "s_nop 0\n"
    "v_mfma_f32_16x16x32_bf16 v[16:19], a[92:95], v[192:195], v[16:19]\n"
    "v_cndmask_b32 v208, v222, v221, vcc\n"
    "v_mfma_f32_16x16x32_bf16 v[20:23], a[92:95], v[196:199], v[20:23]\n"
    "s_waitcnt lgkmcnt(14)\n"
    "s_mov_b32 s75, 12288\n"
    "v_mfma_f32_16x16x32_bf16 v[24:27], a[96:99], v[48:51], 0\n"
    "buffer_store_dwordx4 v[164:167], v215, s[60:63], s75 offen offset:2048 sc1\n"
    "v_mfma_f32_16x16x32_bf16 v[28:31], a[96:99], v[52:55], 0\n"
    "v_lshrrev_b32 v218, 6, v217\n"
    "v_mfma_f32_16x16x32_bf16 v[32:35], a[96:99], v[56:59], 0\n"
    "v_mul_u32_u24 v219, 43, v218\n"
    "v_mfma_f32_16x16x32_bf16 v[36:39], a[96:99], v[60:63], 0\n"
    "v_mfma_f32_16x16x32_bf16 v[40:43], a[96:99], v[64:67], 0\n"
    "v_lshrrev_b32 v219, 7, v219\n"
    "v_mfma_f32_16x16x32_bf16 v[44:47], a[96:99], v[68:71], 0\n"
    "s_waitcnt lgkmcnt(12)\n"
    "v_mfma_f32_16x16x32_bf16 v[24:27], a[100:103], v[72:75], v[24:27]\n"
    "v_mul_f32 v0, v200, v0\n"
    "v_mul_f32 v1, v200, v1\n"
    "v_mfma_f32_16x16x32_bf16 v[28:31], a[100:103], v[76:79], v[28:31]\n"
    "v_mul_f32 v2, v200, v2\n"
    "v_mul_u32_u24 v221, 3, v219\n"
    "v_mfma_f32_16x16x32_bf16 v[32:35], a[100:103], v[80:83], v[32:35]\n"
    "ds_read_b64_tr_b16 a[128:129], v213 offset:0\n"
    "v_mul_f32 v3, v200, v3\n"
    "v_mfma_f32_16x16x32_bf16 v[36:39], a[100:103], v[84:87], v[36:39]\n"
    "v_cvt_pk_bf16_f32 v232, v0, v1\n"
    "v_sub_u32 v220, v218, v221\n"
    "v_mfma_f32_16x16x32_bf16 v[40:43], a[100:103], v[88:91], v[40:43]\n"
    "ds_read_b64_tr_b16 a[130:131], v213 offset:2048\n"
    "v_cvt_pk_bf16_f32 v233, v2, v3\n"
    "v_mfma_f32_16x16x32_bf16 v[44:47], a[100:103], v[92:95], v[44:47]\n"
    "s_waitcnt lgkmcnt(12)\n"
    "v_mul_f32 v4, v201, v4\n"
    "v_mul_f32 v5, v201, v5\n"
    "v_mfma_f32_16x16x32_bf16 v[24:27], a[104:107], v[96:99], v[24:27]\n"
    "ds_read_b64_tr_b16 a[132:133], v213 offset:4096\n"
    "v_mul_f32 v6, v201, v6\n"
    "v_mfma_f32_16x16x32_bf16 v[28:31], a[104:107], v[100:103], v[28:31]\n"
    "v_mul_f32 v7, v201, v7\n"
    "v_and_b32 v221, 15, v217\n"
    "v_mfma_f32_16x16x32_bf16 v[32:35], a[104:107], v[104:107], v[32:35]\n"
    "ds_read_b64_tr_b16 a[134:135], v213 offset:6144\n"
    "v_cvt_pk_bf16_f32 v234, v4, v5\n"
    "v_mfma_f32_16x16x32_bf16 v[36:39], a[104:107], v[108:111], v[36:39]\n"
    "v_cvt_pk_bf16_f32 v235, v6, v7\n"
    "v_lshl_add_u32 v220, v220, 5, v221\n"
    "v_mfma_f32_16x16x32_bf16 v[40:43], a[104:107], v[112:115], v[40:43]\n"
    "v_mul_f32 v8, v202, v8\n"
    "v_mul_f32 v9, v202, v9\n"
    "v_mfma_f32_16x16x32_bf16 v[44:47], a[104:107], v[116:119], v[44:47]\n"
    "s_waitcnt lgkmcnt(12)\n"
    "ds_read_b64_tr_b16 a[136:137], v213 offset:8192\n"
    "v_mul_f32 v10, v202, v10\n"
    "v_mfma_f32_16x16x32_bf16 v[24:27], a[108:111], v[120:123], v[24:27]\n"
    "ds_read_b64_tr_b16 a[138:139], v213 offset:10240\n"
    "v_mul_f32 v11, v202, v11\n"
    "v_mfma_f32_16x16x32_bf16 v[28:31], a[108:111], v[124:127], v[28:31]\n"
    "s_mov_b32 s75, 24576\n"
    "v_cvt_pk_bf16_f32 v236, v8, v9\n"
    "v_mul_u32_u24 v220, 896, v220\n"
    "v_mfma_f32_16x16x32_bf16 v[32:35], a[108:111], v[128:131], v[32:35]\n"
    "buffer_store_dwordx4 v[232:235], v215, s[60:63], s75 offen offset:0 sc1\n"
    "v_mfma_f32_16x16x32_bf16 v[36:39], a[108:111], v[132:135], v[36:39]\n"
    "v_cvt_pk_bf16_f32 v237, v10, v11\n"
    "v_mul_f32 v12, v203, v12\n"
    "v_mfma_f32_16x16x32_bf16 v[40:43], a[108:111], v[136:139], v[40:43]\n"
    "v_mul_f32 v13, v203, v13\n"
    "v_mul_f32 v14, v203, v14\n"
    "v_mfma_f32_16x16x32_bf16 v[44:47], a[108:111], v[140:143], v[44:47]\n"
    "s_waitcnt lgkmcnt(12)\n"
    "ds_read_b64_tr_b16 a[140:141], v213 offset:12288\n"
    "v_mul_f32 v15, v203, v15\n"
    "v_mfma_f32_16x16x32_bf16 v[24:27], a[112:115], a[192:195], v[24:27]\n"
    "ds_read_b64_tr_b16 a[142:143], v213 offset:14336\n"
    "v_cvt_pk_bf16_f32 v238, v12, v13\n"
    "v_mfma_f32_16x16x32_bf16 v[28:31], a[112:115], a[196:199], v[28:31]\n"
    "ds_read_b64_tr_b16 a[144:145], v213 offset:16384\n"
    "v_cvt_pk_bf16_f32 v239, v14, v15\n"
    "v_mfma_f32_16x16x32_bf16 v[32:35], a[112:115], a[200:203], v[32:35]\n"
    "v_mul_f32 v16, v204, v16\n"
    "v_mul_f32 v17, v204, v17\n"
    "v_mfma_f32_16x16x32_bf16 v[36:39], a[112:115], a[204:207], v[36:39]\n"
    "ds_read_b64_tr_b16 a[146:147], v213 offset:18432\n"
    "v_mul_f32 v18, v204, v18\n"
    "v_mfma_f32_16x16x32_bf16 v[40:43], a[112:115], a[208:211], v[40:43]\n"
    "ds_read_b64_tr_b16 a[148:149], v213 offset:20480\n"
    "v_mul_f32 v19, v204, v19\n"
    "v_mfma_f32_16x16x32_bf16 v[44:47], a[112:115], a[212:215], v[44:47]\n"
    "s_waitcnt lgkmcnt(15)\n"
    "v_cvt_pk_bf16_f32 v240, v16, v17\n"
    "v_lshl_add_u32 v220, v219, 5, v220\n"
    "v_mfma_f32_16x16x32_bf16 v[24:27], a[116:119], a[216:219], v[24:27]\n"
    "ds_read_b64_tr_b16 a[150:151], v213 offset:22528\n"
    "v_cvt_pk_bf16_f32 v241, v18, v19\n"
    "v_mfma_f32_16x16x32_bf16 v[28:31], a[116:119], a[220:223], v[28:31]\n"
    "s_mov_b32 s75, 24576\n"
    "v_mul_f32 v20, v205, v20\n"
    "v_mul_f32 v21, v205, v21\n"
    "v_mfma_f32_16x16x32_bf16 v[32:35], a[116:119], a[224:227], v[32:35]\n"
    "buffer_store_dwordx4 v[236:239], v215, s[60:63], s75 offen offset:1024 sc1\n"
    "v_mfma_f32_16x16x32_bf16 v[36:39], a[116:119], a[228:231], v[36:39]\n"
    "v_mul_f32 v22, v205, v22\n"
    "v_mul_f32 v23, v205, v23\n"
    "v_mfma_f32_16x16x32_bf16 v[40:43], a[116:119], a[232:235], v[40:43]\n"
    "ds_read_b64_tr_b16 a[152:153], v213 offset:24576\n"
    "v_cvt_pk_bf16_f32 v242, v20, v21\n"
    "v_mfma_f32_16x16x32_bf16 v[44:47], a[116:119], a[236:239], v[44:47]\n"
    "s_waitcnt lgkmcnt(15)\n"
    "ds_read_b64_tr_b16 a[154:155], v213 offset:26624\n"
    "v_cvt_pk_bf16_f32 v243, v22, v23\n"
    "v_mfma_f32_16x16x32_bf16 v[24:27], a[120:123], a[240:243], v[24:27]\n"
    "ds_read_b64_tr_b16 a[156:157], v213 offset:28672\n"
    "v_bfe_u32 v221, v217, 4, 2\n"
    "v_mfma_f32_16x16x32_bf16 v[28:31], a[120:123], a[244:247], v[28:31]\n"
    "v_lshl_add_u32 v220, v221, 3, v220\n"
    "v_cndmask_b32 v210, v222, v220, vcc\n"
    "v_mfma_f32_16x16x32_bf16 v[32:35], a[120:123], a[248:251], v[32:35]\n"
    "ds_read_b64_tr_b16 a[158:159], v213 offset:30720\n"
    "v_add_u32 v211, 14336, v210\n"
    "v_mfma_f32_16x16x32_bf16 v[36:39], a[120:123], a[252:255], v[36:39]\n"
    "v_and_b32 v217, 31, v216\n"
    "v_cmp_gt_u32 vcc, 24, v217\n"
    "s_add_u32 s74, s74, 3840\n"
    "v_mfma_f32_16x16x32_bf16 v[40:43], a[120:123], v[168:171], v[40:43]\n"
    "v_add_u32 v217, s74, v217\n"
    "v_lshrrev_b32 v221, 5, v216\n"
    "v_mfma_f32_16x16x32_bf16 v[44:47], a[120:123], v[172:175], v[44:47]\n"
    "s_waitcnt lgkmcnt(15)\n"
    "v_mul_u32_u24 v221, 689152, v221\n"
    "v_lshl_add_u32 v221, v217, 4, v221\n"
    "v_mfma_f32_16x16x32_bf16 v[24:27], a[124:127], v[176:179], v[24:27]\n"
    "v_cndmask_b32 v209, v222, v221, vcc\n"
    "v_cmp_gt_u32 vcc, 24, v216\n"
    "v_mfma_f32_16x16x32_bf16 v[28:31], a[124:127], v[180:183], v[28:31]\n"
    "v_mfma_f32_16x16x32_bf16 v[32:35], a[124:127], v[184:187], v[32:35]\n"
    "v_lshrrev_b32 v218, 6, v217\n"
    "v_mfma_f32_16x16x32_bf16 v[36:39], a[124:127], v[188:191], v[36:39]\n"
    "v_mfma_f32_16x16x32_bf16 v[40:43], a[124:127], v[192:195], v[40:43]\n"
    "v_mul_u32_u24 v219, 43, v218\n"
    "v_mfma_f32_16x16x32_bf16 v[44:47], a[124:127], v[196:199], v[44:47]\n"
    "s_waitcnt lgkmcnt(14)\n"
    "s_mov_b32 s75, 24576\n"
    "v_mfma_f32_16x16x32_bf16 v[0:3], a[128:131], v[48:51], 0\n"
    "buffer_store_dwordx4 v[240:243], v215, s[60:63], s75 offen offset:2048 sc1\n"
    "v_mfma_f32_16x16x32_bf16 v[4:7], a[128:131], v[52:55], 0\n"
    "v_lshrrev_b32 v219, 7, v219\n"
    "v_mfma_f32_16x16x32_bf16 v[8:11], a[128:131], v[56:59], 0\n"
    "v_mul_u32_u24 v221, 3, v219\n"
    "v_mfma_f32_16x16x32_bf16 v[12:15], a[128:131], v[60:63], 0\n"
    "v_mfma_f32_16x16x32_bf16 v[16:19], a[128:131], v[64:67], 0\n"
    "v_sub_u32 v220, v218, v221\n"
    "v_mfma_f32_16x16x32_bf16 v[20:23], a[128:131], v[68:71], 0\n"
    "s_waitcnt lgkmcnt(12)\n"
    "v_mfma_f32_16x16x32_bf16 v[0:3], a[132:135], v[72:75], v[0:3]\n"
    "s_waitcnt vmcnt(6)\n"
    "s_barrier\n"
    "v_mul_f32 v24, v200, v24\n"
    "v_mul_f32 v25, v200, v25\n"
    "v_mfma_f32_16x16x32_bf16 v[4:7], a[132:135], v[76:79], v[4:7]\n"
    "v_mul_f32 v26, v200, v26\n"
    "v_and_b32 v221, 15, v217\n"
    "v_mfma_f32_16x16x32_bf16 v[8:11], a[132:135], v[80:83], v[8:11]\n"
    "ds_read_b64_tr_b16 a[160:161], v212 offset:0\n"
    "v_mul_f32 v27, v200, v27\n"
    "v_mfma_f32_16x16x32_bf16 v[12:15], a[132:135], v[84:87], v[12:15]\n"
    "v_cvt_pk_bf16_f32 v244, v24, v25\n"
    "v_lshl_add_u32 v220, v220, 5, v221\n"
    "v_mfma_f32_16x16x32_bf16 v[16:19], a[132:135], v[88:91], v[16:19]\n"
    "ds_read_b64_tr_b16 a[162:163], v212 offset:2048\n"
    "v_cvt_pk_bf16_f32 v245, v26, v27\n"
    "v_mfma_f32_16x16x32_bf16 v[20:23], a[132:135], v[92:95], v[20:23]\n"
    "s_waitcnt lgkmcnt(12)\n"
    "v_mul_f32 v28, v201, v28\n"
    "v_mul_f32 v29, v201, v29\n"
    "v_mfma_f32_16x16x32_bf16 v[0:3], a[136:139], v[96:99], v[0:3]\n"
    "ds_read_b64_tr_b16 a[164:165], v212 offset:4096\n"
    "v_mul_f32 v30, v201, v30\n"
    "v_mfma_f32_16x16x32_bf16 v[4:7], a[136:139], v[100:103], v[4:7]\n"
    "v_mul_f32 v31, v201, v31\n"
    "v_mul_u32_u24 v220, 896, v220\n"
    "v_mfma_f32_16x16x32_bf16 v[8:11], a[136:139], v[104:107], v[8:11]\n"
    "ds_read_b64_tr_b16 a[166:167], v212 offset:6144\n"
    "v_cvt_pk_bf16_f32 v246, v28, v29\n"
    "v_mfma_f32_16x16x32_bf16 v[12:15], a[136:139], v[108:111], v[12:15]\n"
    "v_cvt_pk_bf16_f32 v247, v30, v31\n"
    "v_lshl_add_u32 v220, v219, 5, v220\n"
    "v_mfma_f32_16x16x32_bf16 v[16:19], a[136:139], v[112:115], v[16:19]\n"
    "v_mul_f32 v32, v202, v32\n"
    "v_mul_f32 v33, v202, v33\n"
    "v_mfma_f32_16x16x32_bf16 v[20:23], a[136:139], v[116:119], v[20:23]\n"
    "s_waitcnt lgkmcnt(12)\n"
    "ds_read_b64_tr_b16 a[168:169], v212 offset:8192\n"
    "v_mul_f32 v34, v202, v34\n"
    "v_mfma_f32_16x16x32_bf16 v[0:3], a[140:143], v[120:123], v[0:3]\n"
    "ds_read_b64_tr_b16 a[170:171], v212 offset:10240\n"
    "v_mul_f32 v35, v202, v35\n"
    "v_mfma_f32_16x16x32_bf16 v[4:7], a[140:143], v[124:127], v[4:7]\n"
    "s_mov_b32 s75, 36864\n"
    "v_cvt_pk_bf16_f32 v248, v32, v33\n"
    "v_bfe_u32 v221, v217, 4, 2\n"
    "v_mfma_f32_16x16x32_bf16 v[8:11], a[140:143], v[128:131], v[8:11]\n"
    "buffer_store_dwordx4 v[244:247], v215, s[60:63], s75 offen offset:0 sc1\n"
    "v_mfma_f32_16x16x32_bf16 v[12:15], a[140:143], v[132:135], v[12:15]\n"
    "v_cvt_pk_bf16_f32 v249, v34, v35\n"
    "v_mul_f32 v36, v203, v36\n"
    "v_mfma_f32_16x16x32_bf16 v[16:19], a[140:143], v[136:139], v[16:19]\n"
    "v_mul_f32 v37, v203, v37\n"
    "v_mul_f32 v38, v203, v38\n"
    "v_mfma_f32_16x16x32_bf16 v[20:23], a[140:143], v[140:143], v[20:23]\n"
    "s_waitcnt lgkmcnt(12)\n"
    "ds_read_b64_tr_b16 a[172:173], v212 offset:12288\n"
    "v_mul_f32 v39, v203, v39\n"
    "v_mfma_f32_16x16x32_bf16 v[0:3], a[144:147], a[192:195], v[0:3]\n"
    "ds_read_b64_tr_b16 a[174:175], v212 offset:14336\n"
    "v_cvt_pk_bf16_f32 v250, v36, v37\n"
    "v_mfma_f32_16x16x32_bf16 v[4:7], a[144:147], a[196:199], v[4:7]\n"
    "ds_read_b64_tr_b16 a[176:177], v212 offset:16384\n"
    "v_cvt_pk_bf16_f32 v251, v38, v39\n"
    "v_mfma_f32_16x16x32_bf16 v[8:11], a[144:147], a[200:203], v[8:11]\n"
    "v_mul_f32 v40, v204, v40\n"
    "v_mul_f32 v41, v204, v41\n"
    "v_mfma_f32_16x16x32_bf16 v[12:15], a[144:147], a[204:207], v[12:15]\n"
    "ds_read_b64_tr_b16 a[178:179], v212 offset:18432\n"
    "v_mul_f32 v42, v204, v42\n"
    "v_mfma_f32_16x16x32_bf16 v[16:19], a[144:147], a[208:211], v[16:19]\n"
    "ds_read_b64_tr_b16 a[180:181], v212 offset:20480\n"
    "v_mul_f32 v43, v204, v43\n"
    "v_mfma_f32_16x16x32_bf16 v[20:23], a[144:147], a[212:215], v[20:23]\n"
    "s_waitcnt lgkmcnt(15)\n"
    "v_cvt_pk_bf16_f32 v252, v40, v41\n"
    "v_lshl_add_u32 v220, v221, 3, v220\n"
    "v_mfma_f32_16x16x32_bf16 v[0:3], a[148:151], a[216:219], v[0:3]\n"
    "ds_read_b64_tr_b16 a[182:183], v212 offset:22528\n"
    "v_cvt_pk_bf16_f32 v253, v42, v43\n"
    "v_mfma_f32_16x16x32_bf16 v[4:7], a[148:151], a[220:223], v[4:7]\n"
    "s_mov_b32 s75, 36864\n"
    "v_mul_f32 v44, v205, v44\n"
    "v_mul_f32 v45, v205, v45\n"
    "v_mfma_f32_16x16x32_bf16 v[8:11], a[148:151], a[224:227], v[8:11]\n"
    "buffer_store_dwordx4 v[248:251], v215, s[60:63], s75 offen offset:1024 sc1\n"
    "v_mfma_f32_16x16x32_bf16 v[12:15], a[148:151], a[228:231], v[12:15]\n"
    "v_mul_f32 v46, v205, v46\n"
    "v_mul_f32 v47, v205, v47\n"
    "v_mfma_f32_16x16x32_bf16 v[16:19], a[148:151], a[232:235], v[16:19]\n"
    "ds_read_b64_tr_b16 a[184:185], v212 offset:24576\n"
    "v_cvt_pk_bf16_f32 v254, v44, v45\n"
    "v_mfma_f32_16x16x32_bf16 v[20:23], a[148:151], a[236:239], v[20:23]\n"
    "s_waitcnt lgkmcnt(15)\n"
    "ds_read_b64_tr_b16 a[186:187], v212 offset:26624\n"
    "v_cvt_pk_bf16_f32 v255, v46, v47\n"
    "v_mfma_f32_16x16x32_bf16 v[0:3], a[152:155], a[240:243], v[0:3]\n"
    "ds_read_b64_tr_b16 a[188:189], v212 offset:28672\n"
    "v_cndmask_b32 v223, v222, v220, vcc\n"
    "v_mfma_f32_16x16x32_bf16 v[4:7], a[152:155], a[244:247], v[4:7]\n"
    "v_add_u32 v224, 14336, v223\n"
    "s_mul_i32 s74, s86, 86144\n"
    "s_sub_u32 s40, s60, s74\n"
    "s_subb_u32 s41, s61, 0\n"
    "s_mov_b32 s42, 1378304\n"
    "s_mov_b64 s[44:45], s[34:35]\n"
    "s_mov_b32 s46, 86016\n"
    "v_nop\n"
    "v_mfma_f32_16x16x32_bf16 v[8:11], a[152:155], a[248:251], v[8:11]\n"
    "ds_read_b64_tr_b16 a[190:191], v212 offset:30720\n"
    "v_mfma_f32_16x16x32_bf16 v[12:15], a[152:155], a[252:255], v[12:15]\n"
    "v_mfma_f32_16x16x32_bf16 v[16:19], a[152:155], v[168:171], v[16:19]\n"
    "v_mfma_f32_16x16x32_bf16 v[20:23], a[152:155], v[172:175], v[20:23]\n"
    "s_waitcnt lgkmcnt(15)\n"
    "v_mfma_f32_16x16x32_bf16 v[0:3], a[156:159], v[176:179], v[0:3]\n"
    "v_mfma_f32_16x16x32_bf16 v[4:7], a[156:159], v[180:183], v[4:7]\n"
    "v_mfma_f32_16x16x32_bf16 v[8:11], a[156:159], v[184:187], v[8:11]\n"
    "v_mfma_f32_16x16x32_bf16 v[12:15], a[156:159], v[188:191], v[12:15]\n"
    "v_mfma_f32_16x16x32_bf16 v[16:19], a[156:159], v[192:195], v[16:19]\n"
    "v_mfma_f32_16x16x32_bf16 v[20:23], a[156:159], v[196:199], v[20:23]\n"
    "s_waitcnt lgkmcnt(14)\n"
    "s_mov_b32 s75, 36864\n"
    "v_mfma_f32_16x16x32_bf16 v[24:27], a[160:163], v[48:51], 0\n"
    "buffer_store_dwordx4 v[252:255], v215, s[60:63], s75 offen offset:2048 sc1\n"
    "v_mfma_f32_16x16x32_bf16 v[28:31], a[160:163], v[52:55], 0\n"
    "v_mfma_f32_16x16x32_bf16 v[32:35], a[160:163], v[56:59], 0\n"
    "v_mfma_f32_16x16x32_bf16 v[36:39], a[160:163], v[60:63], 0\n"
    "v_mfma_f32_16x16x32_bf16 v[40:43], a[160:163], v[64:67], 0\n"
    "v_mfma_f32_16x16x32_bf16 v[44:47], a[160:163], v[68:71], 0\n"
    "s_waitcnt lgkmcnt(12)\n"
    "v_mfma_f32_16x16x32_bf16 v[24:27], a[164:167], v[72:75], v[24:27]\n"
    "v_mul_f32 v0, v200, v0\n"
    "v_mul_f32 v1, v200, v1\n"
    "v_mfma_f32_16x16x32_bf16 v[28:31], a[164:167], v[76:79], v[28:31]\n"
    "v_mul_f32 v2, v200, v2\n"
    "v_mfma_f32_16x16x32_bf16 v[32:35], a[164:167], v[80:83], v[32:35]\n"
    "ds_read_b64_tr_b16 a[0:1], v212 offset:32768\n"
    "v_mul_f32 v3, v200, v3\n"
    "v_mfma_f32_16x16x32_bf16 v[36:39], a[164:167], v[84:87], v[36:39]\n"
    "v_cvt_pk_bf16_f32 v144, v0, v1\n"
    "v_mfma_f32_16x16x32_bf16 v[40:43], a[164:167], v[88:91], v[40:43]\n"
    "ds_read_b64_tr_b16 a[2:3], v212 offset:34816\n"
    "v_cvt_pk_bf16_f32 v145, v2, v3\n"
    "v_mfma_f32_16x16x32_bf16 v[44:47], a[164:167], v[92:95], v[44:47]\n"
    "s_waitcnt lgkmcnt(12)\n"
    "v_mul_f32 v4, v201, v4\n"
    "v_mul_f32 v5, v201, v5\n"
    "v_mfma_f32_16x16x32_bf16 v[24:27], a[168:171], v[96:99], v[24:27]\n"
    "ds_read_b64_tr_b16 a[4:5], v212 offset:36864\n"
    "v_mul_f32 v6, v201, v6\n"
    "v_mfma_f32_16x16x32_bf16 v[28:31], a[168:171], v[100:103], v[28:31]\n"
    "v_mul_f32 v7, v201, v7\n"
    "v_mfma_f32_16x16x32_bf16 v[32:35], a[168:171], v[104:107], v[32:35]\n"
    "ds_read_b64_tr_b16 a[6:7], v212 offset:38912\n"
    "v_cvt_pk_bf16_f32 v146, v4, v5\n"
    "v_mfma_f32_16x16x32_bf16 v[36:39], a[168:171], v[108:111], v[36:39]\n"
    "v_cvt_pk_bf16_f32 v147, v6, v7\n"
    "v_mfma_f32_16x16x32_bf16 v[40:43], a[168:171], v[112:115], v[40:43]\n"
    "v_mul_f32 v8, v202, v8\n"
    "v_mul_f32 v9, v202, v9\n"
    "v_mfma_f32_16x16x32_bf16 v[44:47], a[168:171], v[116:119], v[44:47]\n"
    "s_waitcnt lgkmcnt(12)\n"
    "ds_read_b64_tr_b16 a[8:9], v212 offset:40960\n"
    "v_mul_f32 v10, v202, v10\n"
    "v_mfma_f32_16x16x32_bf16 v[24:27], a[172:175], v[120:123], v[24:27]\n"
    "ds_read_b64_tr_b16 a[10:11], v212 offset:43008\n"
    "v_mul_f32 v11, v202, v11\n"
    "v_mfma_f32_16x16x32_bf16 v[28:31], a[172:175], v[124:127], v[28:31]\n"
    "s_mov_b32 s75, 49152\n"
    "v_cvt_pk_bf16_f32 v148, v8, v9\n"
    "v_mfma_f32_16x16x32_bf16 v[32:35], a[172:175], v[128:131], v[32:35]\n"
    "buffer_store_dwordx4 v[144:147], v215, s[60:63], s75 offen offset:0 sc1\n"
    "v_mfma_f32_16x16x32_bf16 v[36:39], a[172:175], v[132:135], v[36:39]\n"
    "v_cvt_pk_bf16_f32 v149, v10, v11\n"
    "v_mul_f32 v12, v203, v12\n"
    "v_mfma_f32_16x16x32_bf16 v[40:43], a[172:175], v[136:139], v[40:43]\n"
    "v_mul_f32 v13, v203, v13\n"
    "v_mul_f32 v14, v203, v14\n"
    "v_mfma_f32_16x16x32_bf16 v[44:47], a[172:175], v[140:143], v[44:47]\n"
    "s_waitcnt lgkmcnt(12)\n"
    "ds_read_b64_tr_b16 a[12:13], v212 offset:45056\n"
    "v_mul_f32 v15, v203, v15\n"
    "v_mfma_f32_16x16x32_bf16 v[24:27], a[176:179], a[192:195], v[24:27]\n"
    "ds_read_b64_tr_b16 a[14:15], v212 offset:47104\n"
    "v_cvt_pk_bf16_f32 v150, v12, v13\n"
    "v_mfma_f32_16x16x32_bf16 v[28:31], a[176:179], a[196:199], v[28:31]\n"
    "ds_read_b64_tr_b16 a[16:17], v212 offset:49152\n"
    "v_cvt_pk_bf16_f32 v151, v14, v15\n"
    "v_mfma_f32_16x16x32_bf16 v[32:35], a[176:179], a[200:203], v[32:35]\n"
    "v_mul_f32 v16, v204, v16\n"
    "v_mul_f32 v17, v204, v17\n"
    "v_mfma_f32_16x16x32_bf16 v[36:39], a[176:179], a[204:207], v[36:39]\n"
    "ds_read_b64_tr_b16 a[18:19], v212 offset:51200\n"
    "v_mul_f32 v18, v204, v18\n"
    "v_mfma_f32_16x16x32_bf16 v[40:43], a[176:179], a[208:211], v[40:43]\n"
    "ds_read_b64_tr_b16 a[20:21], v212 offset:53248\n"
    "v_mul_f32 v19, v204, v19\n"
    "v_mfma_f32_16x16x32_bf16 v[44:47], a[176:179], a[212:215], v[44:47]\n"
    "s_waitcnt lgkmcnt(15)\n"
    "v_cvt_pk_bf16_f32 v152, v16, v17\n"
    "v_mfma_f32_16x16x32_bf16 v[24:27], a[180:183], a[216:219], v[24:27]\n"
    "ds_read_b64_tr_b16 a[22:23], v212 offset:55296\n"
    "v_cvt_pk_bf16_f32 v153, v18, v19\n"
    "v_mfma_f32_16x16x32_bf16 v[28:31], a[180:183], a[220:223], v[28:31]\n"
    "s_mov_b32 s75, 49152\n"
    "v_mul_f32 v20, v205, v20\n"
    "v_mul_f32 v21, v205, v21\n"
    "v_mfma_f32_16x16x32_bf16 v[32:35], a[180:183], a[224:227], v[32:35]\n"
    "buffer_store_dwordx4 v[148:151], v215, s[60:63], s75 offen offset:1024 sc1\n"
    "v_mfma_f32_16x16x32_bf16 v[36:39], a[180:183], a[228:231], v[36:39]\n"
    "v_mul_f32 v22, v205, v22\n"
    "v_mul_f32 v23, v205, v23\n"
    "v_mfma_f32_16x16x32_bf16 v[40:43], a[180:183], a[232:235], v[40:43]\n"
    "ds_read_b64_tr_b16 a[24:25], v212 offset:57344\n"
    "v_cvt_pk_bf16_f32 v154, v20, v21\n"
    "v_mfma_f32_16x16x32_bf16 v[44:47], a[180:183], a[236:239], v[44:47]\n"
    "s_waitcnt lgkmcnt(15)\n"
    "ds_read_b64_tr_b16 a[26:27], v212 offset:59392\n"
    "v_cvt_pk_bf16_f32 v155, v22, v23\n"
    "v_mfma_f32_16x16x32_bf16 v[24:27], a[184:187], a[240:243], v[24:27]\n"
    "ds_read_b64_tr_b16 a[28:29], v212 offset:61440\n"
    "v_mfma_f32_16x16x32_bf16 v[28:31], a[184:187], a[244:247], v[28:31]\n"
    "v_mfma_f32_16x16x32_bf16 v[32:35], a[184:187], a[248:251], v[32:35]\n"
    "ds_read_b64_tr_b16 a[30:31], v212 offset:63488\n"
    "v_mfma_f32_16x16x32_bf16 v[36:39], a[184:187], a[252:255], v[36:39]\n"
    "v_mfma_f32_16x16x32_bf16 v[40:43], a[184:187], v[168:171], v[40:43]\n"
    "v_mfma_f32_16x16x32_bf16 v[44:47], a[184:187], v[172:175], v[44:47]\n"
    "s_waitcnt lgkmcnt(15)\n"
    "v_mfma_f32_16x16x32_bf16 v[24:27], a[188:191], v[176:179], v[24:27]\n"
    "v_mfma_f32_16x16x32_bf16 v[28:31], a[188:191], v[180:183], v[28:31]\n"
    "v_mfma_f32_16x16x32_bf16 v[32:35], a[188:191], v[184:187], v[32:35]\n"
    "v_mfma_f32_16x16x32_bf16 v[36:39], a[188:191], v[188:191], v[36:39]\n"
    "v_mfma_f32_16x16x32_bf16 v[40:43], a[188:191], v[192:195], v[40:43]\n"
    "v_mfma_f32_16x16x32_bf16 v[44:47], a[188:191], v[196:199], v[44:47]\n"
    "s_waitcnt lgkmcnt(14)\n"
    "s_mov_b32 s75, 49152\n"
    "v_mfma_f32_16x16x32_bf16 v[0:3], a[0:3], v[48:51], 0\n"
    "buffer_store_dwordx4 v[152:155], v215, s[60:63], s75 offen offset:2048 sc1\n"
    "v_mfma_f32_16x16x32_bf16 v[4:7], a[0:3], v[52:55], 0\n"
    "v_mfma_f32_16x16x32_bf16 v[8:11], a[0:3], v[56:59], 0\n"
    "v_mfma_f32_16x16x32_bf16 v[12:15], a[0:3], v[60:63], 0\n"
    "v_mfma_f32_16x16x32_bf16 v[16:19], a[0:3], v[64:67], 0\n"
    "v_mfma_f32_16x16x32_bf16 v[20:23], a[0:3], v[68:71], 0\n"
    "s_waitcnt lgkmcnt(12)\n"
    "v_mfma_f32_16x16x32_bf16 v[0:3], a[4:7], v[72:75], v[0:3]\n"
    "v_mul_f32 v24, v200, v24\n"
    "v_mul_f32 v25, v200, v25\n"
    "v_mfma_f32_16x16x32_bf16 v[4:7], a[4:7], v[76:79], v[4:7]\n"
    "v_mul_f32 v26, v200, v26\n"
    "v_mfma_f32_16x16x32_bf16 v[8:11], a[4:7], v[80:83], v[8:11]\n"
    "v_mul_f32 v27, v200, v27\n"
    "v_mfma_f32_16x16x32_bf16 v[12:15], a[4:7], v[84:87], v[12:15]\n"
    "v_cvt_pk_bf16_f32 v156, v24, v25\n"
    "v_mfma_f32_16x16x32_bf16 v[16:19], a[4:7], v[88:91], v[16:19]\n"
    "v_cvt_pk_bf16_f32 v157, v26, v27\n"
    "v_mfma_f32_16x16x32_bf16 v[20:23], a[4:7], v[92:95], v[20:23]\n"
    "s_waitcnt lgkmcnt(10)\n"
    "v_mul_f32 v28, v201, v28\n"
    "v_mul_f32 v29, v201, v29\n"
    "v_mfma_f32_16x16x32_bf16 v[0:3], a[8:11], v[96:99], v[0:3]\n"
    "v_mul_f32 v30, v201, v30\n"
    "v_mfma_f32_16x16x32_bf16 v[4:7], a[8:11], v[100:103], v[4:7]\n"
    "v_mul_f32 v31, v201, v31\n"
    "v_mfma_f32_16x16x32_bf16 v[8:11], a[8:11], v[104:107], v[8:11]\n"
    "v_cvt_pk_bf16_f32 v158, v28, v29\n"
    "v_mfma_f32_16x16x32_bf16 v[12:15], a[8:11], v[108:111], v[12:15]\n"
    "v_cvt_pk_bf16_f32 v159, v30, v31\n"
    "v_mfma_f32_16x16x32_bf16 v[16:19], a[8:11], v[112:115], v[16:19]\n"
    "v_mul_f32 v32, v202, v32\n"
    "v_mul_f32 v33, v202, v33\n"
    "v_mfma_f32_16x16x32_bf16 v[20:23], a[8:11], v[116:119], v[20:23]\n"
    "s_waitcnt lgkmcnt(8)\n"
    "v_mul_f32 v34, v202, v34\n"
    "v_mfma_f32_16x16x32_bf16 v[0:3], a[12:15], v[120:123], v[0:3]\n"
    "v_mul_f32 v35, v202, v35\n"
    "v_mfma_f32_16x16x32_bf16 v[4:7], a[12:15], v[124:127], v[4:7]\n"
    "s_mov_b32 s75, 61440\n"
    "v_cvt_pk_bf16_f32 v160, v32, v33\n"
    "v_mfma_f32_16x16x32_bf16 v[8:11], a[12:15], v[128:131], v[8:11]\n"
    "buffer_store_dwordx4 v[156:159], v215, s[60:63], s75 offen offset:0 sc1\n"
    "v_mfma_f32_16x16x32_bf16 v[12:15], a[12:15], v[132:135], v[12:15]\n"
    "v_cvt_pk_bf16_f32 v161, v34, v35\n"
    "v_mul_f32 v36, v203, v36\n"
    "v_mfma_f32_16x16x32_bf16 v[16:19], a[12:15], v[136:139], v[16:19]\n"
    "v_mul_f32 v37, v203, v37\n"
    "v_mul_f32 v38, v203, v38\n"
    "v_mfma_f32_16x16x32_bf16 v[20:23], a[12:15], v[140:143], v[20:23]\n"
    "s_waitcnt lgkmcnt(6)\n"
    "v_mul_f32 v39, v203, v39\n"
    "v_mfma_f32_16x16x32_bf16 v[0:3], a[16:19], a[192:195], v[0:3]\n"
    "v_cvt_pk_bf16_f32 v162, v36, v37\n"
    "v_mfma_f32_16x16x32_bf16 v[4:7], a[16:19], a[196:199], v[4:7]\n"
    "v_cvt_pk_bf16_f32 v163, v38, v39\n"
    "v_mfma_f32_16x16x32_bf16 v[8:11], a[16:19], a[200:203], v[8:11]\n"
    "v_mul_f32 v40, v204, v40\n"
    "v_mul_f32 v41, v204, v41\n"
    "v_mfma_f32_16x16x32_bf16 v[12:15], a[16:19], a[204:207], v[12:15]\n"
    "v_mul_f32 v42, v204, v42\n"
    "v_mfma_f32_16x16x32_bf16 v[16:19], a[16:19], a[208:211], v[16:19]\n"
    "v_mul_f32 v43, v204, v43\n"
    "v_mfma_f32_16x16x32_bf16 v[20:23], a[16:19], a[212:215], v[20:23]\n"
    "s_waitcnt lgkmcnt(4)\n"
    "v_cvt_pk_bf16_f32 v164, v40, v41\n"
    "v_mfma_f32_16x16x32_bf16 v[0:3], a[20:23], a[216:219], v[0:3]\n"
    "v_cvt_pk_bf16_f32 v165, v42, v43\n"
    "v_mfma_f32_16x16x32_bf16 v[4:7], a[20:23], a[220:223], v[4:7]\n"
    "s_mov_b32 s75, 61440\n"
    "v_mul_f32 v44, v205, v44\n"
    "v_mul_f32 v45, v205, v45\n"
    "v_mfma_f32_16x16x32_bf16 v[8:11], a[20:23], a[224:227], v[8:11]\n"
    "buffer_store_dwordx4 v[160:163], v215, s[60:63], s75 offen offset:1024 sc1\n"
    "v_mfma_f32_16x16x32_bf16 v[12:15], a[20:23], a[228:231], v[12:15]\n"
    "v_mul_f32 v46, v205, v46\n"
    "v_mul_f32 v47, v205, v47\n"
    "v_mfma_f32_16x16x32_bf16 v[16:19], a[20:23], a[232:235], v[16:19]\n"
    "v_cvt_pk_bf16_f32 v166, v44, v45\n"
    "v_mfma_f32_16x16x32_bf16 v[20:23], a[20:23], a[236:239], v[20:23]\n"
    "s_waitcnt lgkmcnt(2)\n"
    "v_cvt_pk_bf16_f32 v167, v46, v47\n"
    "v_mfma_f32_16x16x32_bf16 v[0:3], a[24:27], a[240:243], v[0:3]\n"
    "v_mfma_f32_16x16x32_bf16 v[4:7], a[24:27], a[244:247], v[4:7]\n"
    "v_mfma_f32_16x16x32_bf16 v[8:11], a[24:27], a[248:251], v[8:11]\n"
    "v_mfma_f32_16x16x32_bf16 v[12:15], a[24:27], a[252:255], v[12:15]\n"
    "v_mfma_f32_16x16x32_bf16 v[16:19], a[24:27], v[168:171], v[16:19]\n"
    "v_mfma_f32_16x16x32_bf16 v[20:23], a[24:27], v[172:175], v[20:23]\n"
    "s_waitcnt lgkmcnt(0)\n"
    "v_mfma_f32_16x16x32_bf16 v[0:3], a[28:31], v[176:179], v[0:3]\n"
    "v_mfma_f32_16x16x32_bf16 v[4:7], a[28:31], v[180:183], v[4:7]\n"
    "v_mfma_f32_16x16x32_bf16 v[8:11], a[28:31], v[184:187], v[8:11]\n"
    "v_mfma_f32_16x16x32_bf16 v[12:15], a[28:31], v[188:191], v[12:15]\n"
    "s_mov_b32 s75, 61440\n"
    "v_mfma_f32_16x16x32_bf16 v[16:19], a[28:31], v[192:195], v[16:19]\n"
    "buffer_store_dwordx4 v[164:167], v215, s[60:63], s75 offen offset:2048 sc1\n"
    "v_mfma_f32_16x16x32_bf16 v[20:23], a[28:31], v[196:199], v[20:23]\n"
    "s_nop 7\n"
    "v_mul_f32 v0, v200, v0\n"
    "v_mul_f32 v1, v200, v1\n"
    "v_mul_f32 v2, v200, v2\n"
    "v_mul_f32 v3, v200, v3\n"
    "v_cvt_pk_bf16_f32 v232, v0, v1\n"
    "v_cvt_pk_bf16_f32 v233, v2, v3\n"
    "v_mul_f32 v4, v201, v4\n"
    "v_mul_f32 v5, v201, v5\n"
    "v_mul_f32 v6, v201, v6\n"
    "v_mul_f32 v7, v201, v7\n"
    "v_cvt_pk_bf16_f32 v234, v4, v5\n"
    "v_cvt_pk_bf16_f32 v235, v6, v7\n"
    "s_mov_b32 s75, 73728\n"
    "buffer_store_dwordx4 v[232:235], v215, s[60:63], s75 offen offset:0 sc1\n"
    "v_mul_f32 v8, v202, v8\n"
    "v_mul_f32 v9, v202, v9\n"
    "v_mul_f32 v10, v202, v10\n"
    "v_mul_f32 v11, v202, v11\n"
    "v_cvt_pk_bf16_f32 v236, v8, v9\n"
    "v_cvt_pk_bf16_f32 v237, v10, v11\n"
    "v_mul_f32 v12, v203, v12\n"
    "v_mul_f32 v13, v203, v13\n"
    "v_mul_f32 v14, v203, v14\n"
    "v_mul_f32 v15, v203, v15\n"
    "v_cvt_pk_bf16_f32 v238, v12, v13\n"
    "v_cvt_pk_bf16_f32 v239, v14, v15\n"
    "s_mov_b32 s75, 73728\n"
    "buffer_store_dwordx4 v[236:239], v215, s[60:63], s75 offen offset:1024 sc1\n"
    "v_mul_f32 v16, v204, v16\n"
    "v_mul_f32 v17, v204, v17\n"
    "v_mul_f32 v18, v204, v18\n"
    "v_mul_f32 v19, v204, v19\n"
    "v_cvt_pk_bf16_f32 v240, v16, v17\n"
    "v_cvt_pk_bf16_f32 v241, v18, v19\n"
    "v_mul_f32 v20, v205, v20\n"
    "v_mul_f32 v21, v205, v21\n"
    "v_mul_f32 v22, v205, v22\n"
    "v_mul_f32 v23, v205, v23\n"
    "v_cvt_pk_bf16_f32 v242, v20, v21\n"
    "v_cvt_pk_bf16_f32 v243, v22, v23\n"
    "s_mov_b32 s75, 73728\n"
    "buffer_store_dwordx4 v[240:243], v215, s[60:63], s75 offen offset:2048 sc1\n"
    "v_mov_b32 v168, v208\n"
    "v_mov_b32 v169, v209\n"
    "v_mov_b32 v170, v210\n"
    "v_mov_b32 v171, v211\n"
    "v_mov_b32 v172, v223\n"
    "v_mov_b32 v173, v224\n"
    "s_cmp_lg_u64 s[72:73], 0\n"
    "s_cbranch_scc1 LEXIT_%=\n"
    "s_and_b32 s75, s94, 0xffffffc0\n"
    "s_mov_b32 s65, 0\n"
    "s_mov_b32 s66, 0\n"
    "s_mov_b32 s70, 0\n"
    "s_mov_b32 s71, 0\n"
    "v_mov_b32 v174, s89\n"
    "v_add_u32 v175, 1, v174\n"
    "s_cmp_lg_u32 s83, 384\n"
    "s_waitcnt vmcnt(6)\n"
    "s_barrier\n"
    "s_cbranch_scc1 PS2F_%=\n"
    "buffer_store_dword v175, v229, s[96:99], 0 offen sc1\n"
    "PS2F_%=:\n"
    "v_add_u32 v174, 2, v174\n"
    "v_mov_b32 v176, 0\n"
    "v_mov_b32 v177, 0\n"
    "v_mov_b32 v178, 0\n"
    "v_mov_b32 v179, 0\n"
    "v_mov_b32 v180, 0\n"
    "v_mov_b32 v181, 0\n"
    "v_mov_b32 v182, 0\n"
    "v_mov_b32 v183, 0\n"
    "v_mbcnt_lo_u32_b32 v184, -1, 0\n"
    "v_mbcnt_hi_u32_b32 v184, -1, v184\n"
    "v_lshrrev_b32 v185, 4, v184\n"
    "v_bfe_u32 v186, v184, 2, 2\n"
    "v_and_b32 v187, 1, v184\n"
    "v_cmp_eq_u32 vcc, v185, v186\n"
    "v_lshlrev_b32 v187, 4, v187\n"
    "v_mov_b32 v185, 0x3f80\n"
    "v_lshlrev_b32 v187, v187, v185\n"
    "v_cndmask_b32 v187, 0, v187, vcc\n"
    "v_and_b32 v186, 2, v184\n"
    "v_cmp_eq_u32 vcc, 0, v186\n"
    "v_mov_b32 v194, 0\n"
    "v_mov_b32 v195, 0\n"
    "v_cndmask_b32 v196, 0, v187, vcc\n"
    "v_sub_u32 v197, v187, v196\n"
    "v_mov_b32 v198, 0\n"
    "v_mov_b32 v199, 0\n"
    "s_cmp_lg_u32 s83, 384\n"
    "s_waitcnt vmcnt(0)\n"
    "s_barrier\n"
    "s_cbranch_scc1 PL0F_%=\n"
    "buffer_store_dword v174, v229, s[96:99], 0 offen sc1\n"
    "PL0F_%=:\n"
    "PLOOPF_%=:\n"
    "buffer_load_dword v225, v228, s[96:99], s75 offen sc1\n"
    "s_add_u32 s71, s71, 1\n"
    "s_mov_b32 s76, s65\n"
    "s_waitcnt vmcnt(0)\n"
    "v_sub_u32 v184, v225, v207\n"
    "v_cmp_eq_u32 vcc, 2, v184\n"
    "v_add_u32 v185, -1, v184\n"
    "s_lshr_b32 s69, vcc_lo, 8\n"
    "s_and_b32 s69, s69, vcc_lo\n"
    "v_cmp_ge_u32 vcc, 1, v185\n"
    "s_and_b32 s69, s69, 0xff\n"
    "s_andn2_b32 s69, s69, s66\n"
    "s_and_b32 s68, vcc_lo, 0xffff\n"
    "s_andn2_b32 s68, s68, s65\n"
    "s_cmp_eq_u32 s68, 0\n"
    "s_cbranch_scc1 PG1EF_%=\n"
    "s_mov_b32 s77, 0\n"
    "s_bitcmp1_b32 s68, 0\n"
    "s_cbranch_scc0 PG10F_%=\n"
    "buffer_load_dwordx4 v[48:51], v168, s[40:43], s77 offen sc1\n"
    "PG10F_%=:\n"
    "s_mov_b32 s77, 86144\n"
    "s_bitcmp1_b32 s68, 1\n"
    "s_cbranch_scc0 PG11F_%=\n"
    "buffer_load_dwordx4 v[52:55], v168, s[40:43], s77 offen sc1\n"
    "PG11F_%=:\n"
    "s_mov_b32 s77, 172288\n"
    "s_bitcmp1_b32 s68, 2\n"
    "s_cbranch_scc0 PG12F_%=\n"
    "buffer_load_dwordx4 v[56:59], v168, s[40:43], s77 offen sc1\n"
    "PG12F_%=:\n"
    "s_mov_b32 s77, 258432\n"
    "s_bitcmp1_b32 s68, 3\n"
    "s_cbranch_scc0 PG13F_%=\n"
    "buffer_load_dwordx4 v[60:63], v168, s[40:43], s77 offen sc1\n"
    "PG13F_%=:\n"
    "s_mov_b32 s77, 344576\n"
    "s_bitcmp1_b32 s68, 4\n"
    "s_cbranch_scc0 PG14F_%=\n"
    "buffer_load_dwordx4 v[64:67], v168, s[40:43], s77 offen sc1\n"
    "PG14F_%=:\n"
    "s_mov_b32 s77, 430720\n"
    "s_bitcmp1_b32 s68, 5\n"
    "s_cbranch_scc0 PG15F_%=\n"
    "buffer_load_dwordx4 v[68:71], v168, s[40:43], s77 offen sc1\n"
    "PG15F_%=:\n"
    "s_mov_b32 s77, 516864\n"
    "s_bitcmp1_b32 s68, 6\n"
    "s_cbranch_scc0 PG16F_%=\n"
    "buffer_load_dwordx4 v[72:75], v168, s[40:43], s77 offen sc1\n"
    "PG16F_%=:\n"
    "s_mov_b32 s77, 603008\n"
    "s_bitcmp1_b32 s68, 7\n"
    "s_cbranch_scc0 PG17F_%=\n"
    "buffer_load_dwordx4 v[76:79], v168, s[40:43], s77 offen sc1\n"
    "PG17F_%=:\n"
    "s_mov_b32 s77, 689152\n"
    "s_bitcmp1_b32 s68, 8\n"
    "s_cbranch_scc0 PG18F_%=\n"
    "buffer_load_dwordx4 v[80:83], v168, s[40:43], s77 offen sc1\n"
    "PG18F_%=:\n"
    "s_mov_b32 s77, 775296\n"
    "s_bitcmp1_b32 s68, 9\n"
    "s_cbranch_scc0 PG19F_%=\n"
    "buffer_load_dwordx4 v[84:87], v168, s[40:43], s77 offen sc1\n"
    "PG19F_%=:\n"
    "s_mov_b32 s77, 861440\n"
    "s_bitcmp1_b32 s68, 10\n"
    "s_cbranch_scc0 PG110F_%=\n"
    "buffer_load_dwordx4 v[88:91], v168, s[40:43], s77 offen sc1\n"
    "PG110F_%=:\n"
    "s_mov_b32 s77, 947584\n"
    "s_bitcmp1_b32 s68, 11\n"
    "s_cbranch_scc0 PG111F_%=\n"
    "buffer_load_dwordx4 v[92:95], v168, s[40:43], s77 offen sc1\n"
    "PG111F_%=:\n"
    "s_mov_b32 s77, 1033728\n"
    "s_bitcmp1_b32 s68, 12\n"
    "s_cbranch_scc0 PG112F_%=\n"
    "buffer_load_dwordx4 v[96:99], v168, s[40:43], s77 offen sc1\n"
    "PG112F_%=:\n"
    "s_mov_b32 s77, 1119872\n"
    "s_bitcmp1_b32 s68, 13\n"
    "s_cbranch_scc0 PG113F_%=\n"
    "buffer_load_dwordx4 v[100:103], v168, s[40:43], s77 offen sc1\n"
    "PG113F_%=:\n"
    "s_mov_b32 s77, 1206016\n"
    "s_bitcmp1_b32 s68, 14\n"
    "s_cbranch_scc0 PG114F_%=\n"
    "buffer_load_dwordx4 v[104:107], v168, s[40:43], s77 offen sc1\n"
    "PG114F_%=:\n"
    "s_mov_b32 s77, 1292160\n"
    "s_bitcmp1_b32 s68, 15\n"
    "s_cbranch_scc0 PG115F_%=\n"
    "buffer_load_dwordx4 v[108:111], v168, s[40:43], s77 offen sc1\n"
    "PG115F_%=:\n"
    "PG1EF_%=:\n"
    "s_cmp_eq_u32 s69, 0\n"
    "s_cbranch_scc1 PG2EF_%=\n"
    "s_mov_b32 s77, 0\n"
    "s_bitcmp1_b32 s69, 0\n"
    "s_cbranch_scc0 PG20F_%=\n"
    "buffer_load_dwordx4 v[112:115], v169, s[40:43], s77 offen sc1\n"
    "PG20F_%=:\n"
    "s_mov_b32 s77, 86144\n"
    "s_bitcmp1_b32 s69, 1\n"
    "s_cbranch_scc0 PG21F_%=\n"
    "buffer_load_dwordx4 v[116:119], v169, s[40:43], s77 offen sc1\n"
    "PG21F_%=:\n"
    "s_mov_b32 s77, 172288\n"
    "s_bitcmp1_b32 s69, 2\n"
    "s_cbranch_scc0 PG22F_%=\n"
    "buffer_load_dwordx4 v[120:123], v169, s[40:43], s77 offen sc1\n"
    "PG22F_%=:\n"
    "s_mov_b32 s77, 258432\n"
    "s_bitcmp1_b32 s69, 3\n"
    "s_cbranch_scc0 PG23F_%=\n"
    "buffer_load_dwordx4 v[124:127], v169, s[40:43], s77 offen sc1\n"
    "PG23F_%=:\n"
    "s_mov_b32 s77, 344576\n"
    "s_bitcmp1_b32 s69, 4\n"
    "s_cbranch_scc0 PG24F_%=\n"
    "buffer_load_dwordx4 v[128:131], v169, s[40:43], s77 offen sc1\n"
    "PG24F_%=:\n"
    "s_mov_b32 s77, 430720\n"
    "s_bitcmp1_b32 s69, 5\n"
    "s_cbranch_scc0 PG25F_%=\n"
    "buffer_load_dwordx4 v[132:135], v169, s[40:43], s77 offen sc1\n"
    "PG25F_%=:\n"
    "s_mov_b32 s77, 516864\n"
    "s_bitcmp1_b32 s69, 6\n"
    "s_cbranch_scc0 PG26F_%=\n"
    "buffer_load_dwordx4 v[136:139], v169, s[40:43], s77 offen sc1\n"
    "PG26F_%=:\n"
    "s_mov_b32 s77, 603008\n"
    "s_bitcmp1_b32 s69, 7\n"
    "s_cbranch_scc0 PG27F_%=\n"
    "buffer_load_dwordx4 v[140:143], v169, s[40:43], s77 offen sc1\n"
    "PG27F_%=:\n"
    "PG2EF_%=:\n"
    "s_or_b32 s65, s65, s68\n"
    "s_or_b32 s66, s66, s69\n"
    "s_add_u32 s77, s76, 1\n"
    "s_xor_b32 s77, s77, s76\n"
    "s_lshr_b32 s77, s77, 1\n"
    "s_andn2_b32 s68, s77, s70\n"
    "s_mov_b32 s70, s77\n"
    "s_cmp_eq_u32 s68, 0\n"
    "s_cbranch_scc1 PQEF_%=\n"
    "s_bitcmp1_b32 s68, 0\n"
    "s_cbranch_scc0 PQ0F_%=\n"
    "v_mfma_f32_16x16x32_bf16 v[176:179], v[196:199], v[48:51], v[176:179]\n"
    "v_mfma_f32_16x16x32_bf16 v[180:183], v[194:197], v[48:51], v[180:183]\n"
    "PQ0F_%=:\n"
    "s_bitcmp1_b32 s68, 1\n"
    "s_cbranch_scc0 PQ1F_%=\n"
    "v_mfma_f32_16x16x32_bf16 v[176:179], v[196:199], v[52:55], v[176:179]\n"
    "v_mfma_f32_16x16x32_bf16 v[180:183], v[194:197], v[52:55], v[180:183]\n"
    "PQ1F_%=:\n"
    "s_bitcmp1_b32 s68, 2\n"
    "s_cbranch_scc0 PQ2F_%=\n"
    "v_mfma_f32_16x16x32_bf16 v[176:179], v[196:199], v[56:59], v[176:179]\n"
    "v_mfma_f32_16x16x32_bf16 v[180:183], v[194:197], v[56:59], v[180:183]\n"
    "PQ2F_%=:\n"
    "s_bitcmp1_b32 s68, 3\n"
    "s_cbranch_scc0 PQ3F_%=\n"
    "v_mfma_f32_16x16x32_bf16 v[176:179], v[196:199], v[60:63], v[176:179]\n"
    "v_mfma_f32_16x16x32_bf16 v[180:183], v[194:197], v[60:63], v[180:183]\n"
    "PQ3F_%=:\n"
    "s_bitcmp1_b32 s68, 4\n"
    "s_cbranch_scc0 PQ4F_%=\n"
    "v_mfma_f32_16x16x32_bf16 v[176:179], v[196:199], v[64:67], v[176:179]\n"
    "v_mfma_f32_16x16x32_bf16 v[180:183], v[194:197], v[64:67], v[180:183]\n"
    "PQ4F_%=:\n"
    "s_bitcmp1_b32 s68, 5\n"
    "s_cbranch_scc0 PQ5F_%=\n"
    "v_mfma_f32_16x16x32_bf16 v[176:179], v[196:199], v[68:71], v[176:179]\n"
    "v_mfma_f32_16x16x32_bf16 v[180:183], v[194:197], v[68:71], v[180:183]\n"
    "PQ5F_%=:\n"
    "s_bitcmp1_b32 s68, 6\n"
    "s_cbranch_scc0 PQ6F_%=\n"
    "v_mfma_f32_16x16x32_bf16 v[176:179], v[196:199], v[72:75], v[176:179]\n"
    "v_mfma_f32_16x16x32_bf16 v[180:183], v[194:197], v[72:75], v[180:183]\n"
    "PQ6F_%=:\n"
    "s_bitcmp1_b32 s68, 7\n"
    "s_cbranch_scc0 PQ7F_%=\n"
    "v_mfma_f32_16x16x32_bf16 v[176:179], v[196:199], v[76:79], v[176:179]\n"
    "v_mfma_f32_16x16x32_bf16 v[180:183], v[194:197], v[76:79], v[180:183]\n"
    "PQ7F_%=:\n"
    "s_bitcmp1_b32 s68, 8\n"
    "s_cbranch_scc0 PQ8F_%=\n"
    "v_mfma_f32_16x16x32_bf16 v[176:179], v[196:199], v[80:83], v[176:179]\n"
    "v_mfma_f32_16x16x32_bf16 v[180:183], v[194:197], v[80:83], v[180:183]\n"
    "PQ8F_%=:\n"
    "s_bitcmp1_b32 s68, 9\n"
    "s_cbranch_scc0 PQ9F_%=\n"
    "v_mfma_f32_16x16x32_bf16 v[176:179], v[196:199], v[84:87], v[176:179]\n"
    "v_mfma_f32_16x16x32_bf16 v[180:183], v[194:197], v[84:87], v[180:183]\n"
    "PQ9F_%=:\n"
    "s_bitcmp1_b32 s68, 10\n"
    "s_cbranch_scc0 PQ10F_%=\n"
    "v_mfma_f32_16x16x32_bf16 v[176:179], v[196:199], v[88:91], v[176:179]\n"
    "v_mfma_f32_16x16x32_bf16 v[180:183], v[194:197], v[88:91], v[180:183]\n"
    "PQ10F_%=:\n"
    "s_bitcmp1_b32 s68, 11\n"
    "s_cbranch_scc0 PQ11F_%=\n"
    "v_mfma_f32_16x16x32_bf16 v[176:179], v[196:199], v[92:95], v[176:179]\n"
    "v_mfma_f32_16x16x32_bf16 v[180:183], v[194:197], v[92:95], v[180:183]\n"
    "PQ11F_%=:\n"
    "s_bitcmp1_b32 s68, 12\n"
    "s_cbranch_scc0 PQ12F_%=\n"
    "v_mfma_f32_16x16x32_bf16 v[176:179], v[196:199], v[96:99], v[176:179]\n"
    "v_mfma_f32_16x16x32_bf16 v[180:183], v[194:197], v[96:99], v[180:183]\n"
    "PQ12F_%=:\n"
    "s_bitcmp1_b32 s68, 13\n"
    "s_cbranch_scc0 PQ13F_%=\n"
    "v_mfma_f32_16x16x32_bf16 v[176:179], v[196:199], v[100:103], v[176:179]\n"
    "v_mfma_f32_16x16x32_bf16 v[180:183], v[194:197], v[100:103], v[180:183]\n"
    "PQ13F_%=:\n"
    "s_bitcmp1_b32 s68, 14\n"
    "s_cbranch_scc0 PQ14F_%=\n"
    "v_mfma_f32_16x16x32_bf16 v[176:179], v[196:199], v[104:107], v[176:179]\n"
    "v_mfma_f32_16x16x32_bf16 v[180:183], v[194:197], v[104:107], v[180:183]\n"
    "PQ14F_%=:\n"
    "s_bitcmp1_b32 s68, 15\n"
    "s_cbranch_scc0 PQ15F_%=\n"
    "v_mfma_f32_16x16x32_bf16 v[176:179], v[196:199], v[108:111], v[176:179]\n"
    "v_mfma_f32_16x16x32_bf16 v[180:183], v[194:197], v[108:111], v[180:183]\n"
    "PQ15F_%=:\n"
    "PQEF_%=:\n"
    "s_lshl_b32 s77, s66, 16\n"
    "s_or_b32 s77, s77, s65\n"
    "s_cmp_eq_u32 s77, 0xffffff\n"
    "s_cbranch_scc1 PENDF_%=\n"
    "s_cmp_lt_u32 s71, 0x40000\n"
    "s_cbranch_scc1 PLOOPF_%=\n"
    "PENDF_%=:\n"
    "s_waitcnt vmcnt(0)\n"
    "s_add_u32 s77, s65, 1\n"
    "s_xor_b32 s77, s77, s65\n"
    "s_lshr_b32 s77, s77, 1\n"
    "s_andn2_b32 s68, s77, s70\n"
    "s_mov_b32 s70, s77\n"
    "s_cmp_eq_u32 s68, 0\n"
    "s_cbranch_scc1 PREF_%=\n"
    "s_bitcmp1_b32 s68, 0\n"
    "s_cbranch_scc0 PR0F_%=\n"
    "v_mfma_f32_16x16x32_bf16 v[176:179], v[196:199], v[48:51], v[176:179]\n"
    "v_mfma_f32_16x16x32_bf16 v[180:183], v[194:197], v[48:51], v[180:183]\n"
    "PR0F_%=:\n"
    "s_bitcmp1_b32 s68, 1\n"
    "s_cbranch_scc0 PR1F_%=\n"
    "v_mfma_f32_16x16x32_bf16 v[176:179], v[196:199], v[52:55], v[176:179]\n"
    "v_mfma_f32_16x16x32_bf16 v[180:183], v[194:197], v[52:55], v[180:183]\n"
    "PR1F_%=:\n"
    "s_bitcmp1_b32 s68, 2\n"
    "s_cbranch_scc0 PR2F_%=\n"
    "v_mfma_f32_16x16x32_bf16 v[176:179], v[196:199], v[56:59], v[176:179]\n"
    "v_mfma_f32_16x16x32_bf16 v[180:183], v[194:197], v[56:59], v[180:183]\n"
    "PR2F_%=:\n"
    "s_bitcmp1_b32 s68, 3\n"
    "s_cbranch_scc0 PR3F_%=\n"
    "v_mfma_f32_16x16x32_bf16 v[176:179], v[196:199], v[60:63], v[176:179]\n"
    "v_mfma_f32_16x16x32_bf16 v[180:183], v[194:197], v[60:63], v[180:183]\n"
    "PR3F_%=:\n"
    "s_bitcmp1_b32 s68, 4\n"
    "s_cbranch_scc0 PR4F_%=\n"
    "v_mfma_f32_16x16x32_bf16 v[176:179], v[196:199], v[64:67], v[176:179]\n"
    "v_mfma_f32_16x16x32_bf16 v[180:183], v[194:197], v[64:67], v[180:183]\n"
    "PR4F_%=:\n"
    "s_bitcmp1_b32 s68, 5\n"
    "s_cbranch_scc0 PR5F_%=\n"
    "v_mfma_f32_16x16x32_bf16 v[176:179], v[196:199], v[68:71], v[176:179]\n"
    "v_mfma_f32_16x16x32_bf16 v[180:183], v[194:197], v[68:71], v[180:183]\n"
    "PR5F_%=:\n"
    "s_bitcmp1_b32 s68, 6\n"
    "s_cbranch_scc0 PR6F_%=\n"
    "v_mfma_f32_16x16x32_bf16 v[176:179], v[196:199], v[72:75], v[176:179]\n"
    "v_mfma_f32_16x16x32_bf16 v[180:183], v[194:197], v[72:75], v[180:183]\n"
    "PR6F_%=:\n"
    "s_bitcmp1_b32 s68, 7\n"
    "s_cbranch_scc0 PR7F_%=\n"
    "v_mfma_f32_16x16x32_bf16 v[176:179], v[196:199], v[76:79], v[176:179]\n"
    "v_mfma_f32_16x16x32_bf16 v[180:183], v[194:197], v[76:79], v[180:183]\n"
    "PR7F_%=:\n"
    "s_bitcmp1_b32 s68, 8\n"
    "s_cbranch_scc0 PR8F_%=\n"
    "v_mfma_f32_16x16x32_bf16 v[176:179], v[196:199], v[80:83], v[176:179]\n"
    "v_mfma_f32_16x16x32_bf16 v[180:183], v[194:197], v[80:83], v[180:183]\n"
    "PR8F_%=:\n"
    "s_bitcmp1_b32 s68, 9\n"
    "s_cbranch_scc0 PR9F_%=\n"
    "v_mfma_f32_16x16x32_bf16 v[176:179], v[196:199], v[84:87], v[176:179]\n"
    "v_mfma_f32_16x16x32_bf16 v[180:183], v[194:197], v[84:87], v[180:183]\n"
    "PR9F_%=:\n"
    "s_bitcmp1_b32 s68, 10\n"
    "s_cbranch_scc0 PR10F_%=\n"
    "v_mfma_f32_16x16x32_bf16 v[176:179], v[196:199], v[88:91], v[176:179]\n"
    "v_mfma_f32_16x16x32_bf16 v[180:183], v[194:197], v[88:91], v[180:183]\n"
    "PR10F_%=:\n"
    "s_bitcmp1_b32 s68, 11\n"
    "s_cbranch_scc0 PR11F_%=\n"
    "v_mfma_f32_16x16x32_bf16 v[176:179], v[196:199], v[92:95], v[176:179]\n"
    "v_mfma_f32_16x16x32_bf16 v[180:183], v[194:197], v[92:95], v[180:183]\n"
    "PR11F_%=:\n"
    "s_bitcmp1_b32 s68, 12\n"
    "s_cbranch_scc0 PR12F_%=\n"
    "v_mfma_f32_16x16x32_bf16 v[176:179], v[196:199], v[96:99], v[176:179]\n"
    "v_mfma_f32_16x16x32_bf16 v[180:183], v[194:197], v[96:99], v[180:183]\n"
    "PR12F_%=:\n"
    "s_bitcmp1_b32 s68, 13\n"
    "s_cbranch_scc0 PR13F_%=\n"
    "v_mfma_f32_16x16x32_bf16 v[176:179], v[196:199], v[100:103], v[176:179]\n"
    "v_mfma_f32_16x16x32_bf16 v[180:183], v[194:197], v[100:103], v[180:183]\n"
    "PR13F_%=:\n"
    "s_bitcmp1_b32 s68, 14\n"
    "s_cbranch_scc0 PR14F_%=\n"
    "v_mfma_f32_16x16x32_bf16 v[176:179], v[196:199], v[104:107], v[176:179]\n"
    "v_mfma_f32_16x16x32_bf16 v[180:183], v[194:197], v[104:107], v[180:183]\n"
    "PR14F_%=:\n"
    "s_bitcmp1_b32 s68, 15\n"
    "s_cbranch_scc0 PR15F_%=\n"
    "v_mfma_f32_16x16x32_bf16 v[176:179], v[196:199], v[108:111], v[176:179]\n"
    "v_mfma_f32_16x16x32_bf16 v[180:183], v[194:197], v[108:111], v[180:183]\n"
    "PR15F_%=:\n"
    "PREF_%=:\n"
    "s_nop 7\n"
    "v_cvt_pk_bf16_f32 v184, v176, v177\n"
    "v_cvt_pk_bf16_f32 v185, v178, v179\n"
    "v_cvt_pk_bf16_f32 v186, v180, v181\n"
    "v_cvt_pk_bf16_f32 v187, v182, v183\n"
    "buffer_store_dwordx2 v[184:185], v170, s[44:47], 0 offen\n"
    "buffer_store_dwordx2 v[186:187], v171, s[44:47], 0 offen\n"
    "v_mfma_f32_16x16x32_bf16 v[176:179], v[196:199], v[112:115], 0\n"
    "v_mfma_f32_16x16x32_bf16 v[180:183], v[194:197], v[112:115], 0\n"
    "v_mfma_f32_16x16x32_bf16 v[176:179], v[196:199], v[116:119], v[176:179]\n"
    "v_mfma_f32_16x16x32_bf16 v[180:183], v[194:197], v[116:119], v[180:183]\n"
    "v_mfma_f32_16x16x32_bf16 v[176:179], v[196:199], v[120:123], v[176:179]\n"
    "v_mfma_f32_16x16x32_bf16 v[180:183], v[194:197], v[120:123], v[180:183]\n"
    "v_mfma_f32_16x16x32_bf16 v[176:179], v[196:199], v[124:127], v[176:179]\n"
    "v_mfma_f32_16x16x32_bf16 v[180:183], v[194:197], v[124:127], v[180:183]\n"
    "v_mfma_f32_16x16x32_bf16 v[176:179], v[196:199], v[128:131], v[176:179]\n"
    "v_mfma_f32_16x16x32_bf16 v[180:183], v[194:197], v[128:131], v[180:183]\n"
    "v_mfma_f32_16x16x32_bf16 v[176:179], v[196:199], v[132:135], v[176:179]\n"
    "v_mfma_f32_16x16x32_bf16 v[180:183], v[194:197], v[132:135], v[180:183]\n"
    "v_mfma_f32_16x16x32_bf16 v[176:179], v[196:199], v[136:139], v[176:179]\n"
    "v_mfma_f32_16x16x32_bf16 v[180:183], v[194:197], v[136:139], v[180:183]\n"
    "v_mfma_f32_16x16x32_bf16 v[176:179], v[196:199], v[140:143], v[176:179]\n"
    "v_mfma_f32_16x16x32_bf16 v[180:183], v[194:197], v[140:143], v[180:183]\n"
    "s_nop 7\n"
    "v_mov_b32 v188, v176\n"
    "v_mov_b32 v189, v177\n"
    "v_mov_b32 v190, v178\n"
    "v_mov_b32 v191, v179\n"
    "v_mov_b32 v192, v180\n"
    "v_mov_b32 v193, v181\n"
    "v_mov_b32 v194, v182\n"
    "v_mov_b32 v195, v183\n"
    "s_nop 1\n"
    "v_permlane32_swap_b32 v176, v188\n"
    "v_permlane32_swap_b32 v177, v189\n"
    "v_permlane32_swap_b32 v178, v190\n"
    "v_permlane32_swap_b32 v179, v191\n"
    "v_permlane32_swap_b32 v180, v192\n"
    "v_permlane32_swap_b32 v181, v193\n"
    "v_permlane32_swap_b32 v182, v194\n"
    "v_permlane32_swap_b32 v183, v195\n"
    "s_nop 1\n"
    "v_add_f32 v176, v176, v188\n"
    "v_add_f32 v177, v177, v189\n"
    "v_add_f32 v178, v178, v190\n"
    "v_add_f32 v179, v179, v191\n"
    "v_add_f32 v180, v180, v192\n"
    "v_add_f32 v181, v181, v193\n"
    "v_add_f32 v182, v182, v194\n"
    "v_add_f32 v183, v183, v195\n"
    "s_nop 0\n"
    "s_nop 7\n"
    "v_cvt_pk_bf16_f32 v184, v176, v177\n"
    "v_cvt_pk_bf16_f32 v185, v178, v179\n"
    "v_cvt_pk_bf16_f32 v186, v180, v181\n"
    "v_cvt_pk_bf16_f32 v187, v182, v183\n"
    "buffer_store_dwordx2 v[184:185], v172, s[44:47], 0 offen\n"
    "buffer_store_dwordx2 v[186:187], v173, s[44:47], 0 offen\n"
    "LEXIT_%=:\n"
    ".Lgqa_exit:\n"
    : "={s[72:73]}"(bad), "+{v[0:15]}"(pa), "+{v[208:223]}"(pb), "+{v[224:231]}"(pc), "+{v206}"(pd), "+{v[16:19]}"(pe), "+{v[144:147]}"(pf), "={v207}"(vn), "={s89}"(sn),

      "+{s[48:51]}"(sk), "+{s[64:71]}"(sa), "+{s[80:87]}"(sc), "+{s[40:43]}"(sx), "+{s[44:47]}"(sq), "+{s[76:79]}"(sb)
    : "{s[52:55]}"(sv), "{s[56:59]}"(so), "{s[60:63]}"(sp), "{s[34:35]}"((u64)(out + (size_t)b * SQ * DM)),
      "{s94}"(b * FROW + h * 4), "{s[96:99]}"(sf)
    : "v20","v21","v22","v23","v24","v25","v26","v27","v28","v29","v30","v31","v32","v33","v34","v35","v36","v37","v38","v39","v40","v41","v42","v43","v44","v45","v46","v47","v48","v49","v50","v51","v52","v53","v54","v55","v56","v57","v58","v59","v60","v61","v62","v63","v64","v65","v66","v67","v68","v69","v70","v71","v72","v73","v74","v75","v76","v77","v78","v79","v80","v81","v82","v83","v84","v85","v86","v87","v88","v89","v90","v91","v92","v93","v94","v95","v96","v97","v98","v99","v100","v101","v102","v103","v104","v105","v106","v107","v108","v109","v110","v111","v112","v113","v114","v115","v116","v117","v118","v119","v120","v121","v122","v123","v124","v125","v126","v127","v128","v129","v130","v131","v132","v133","v134","v135","v136","v137","v138","v139","v140","v141","v142","v143","v148","v149","v150","v151","v152","v153","v154","v155","v156","v157","v158","v159","v160","v161","v162","v163","v164","v165","v166","v167","v168","v169","v170","v171","v172","v173","v174","v175","v176","v177","v178","v179","v180","v181","v182","v183","v184","v185","v186","v187","v188","v189","v190","v191","v192","v193","v194","v195","v196","v197","v198","v199","v200","v201","v202","v203","v204","v205","v232","v233","v234","v235","v236","v237","v238","v239","v240","v241","v242","v243","v244","v245","v246","v247","v248","v249","v250","v251","v252","v253","v254","v255","a0","a1","a2","a3","a4","a5","a6","a7","a8","a9","a10","a11","a12","a13","a14","a15","a16","a17","a18","a19","a20","a21","a22","a23","a24","a25","a26","a27","a28","a29","a30","a31","a32","a33","a34","a35","a36","a37","a38","a39","a40","a41","a42","a43","a44","a45","a46","a47","a48","a49","a50","a51","a52","a53","a54","a55","a56","a57","a58","a59","a60","a61","a62","a63","a64","a65","a66","a67","a68","a69","a70","a71","a72","a73","a74","a75","a76","a77","a78","a79","a80","a81","a82","a83","a84","a85","a86","a87","a88","a89","a90","a91","a92","a93","a94","a95","a96","a97","a98","a99","a100","a101","a102","a103","a104","a105","a106","a107","a108","a109","a110","a111","a112","a113","a114","a115","a116","a117","a118","a119","a120","a121","a122","a123","a124","a125","a126","a127","a128","a129","a130","a131","a132","a133","a134","a135","a136","a137","a138","a139","a140","a141","a142","a143","a144","a145","a146","a147","a148","a149","a150","a151","a152","a153","a154","a155","a156","a157","a158","a159","a160","a161","a162","a163","a164","a165","a166","a167","a168","a169","a170","a171","a172","a173","a174","a175","a176","a177","a178","a179","a180","a181","a182","a183","a184","a185","a186","a187","a188","a189","a190","a191","a192","a193","a194","a195","a196","a197","a198","a199","a200","a201","a202","a203","a204","a205","a206","a207","a208","a209","a210","a211","a212","a213","a214","a215","a216","a217","a218","a219","a220","a221","a222","a223","a224","a225","a226","a227","a228","a229","a230","a231","a232","a233","a234","a235","a236","a237","a238","a239","a240","a241","a242","a243","a244","a245","a246","a247","a248","a249","a250","a251","a252","a253","a254","a255","s74","s75","s88","s90","s91","s92","s93","s95","vcc","scc","memory");
  u32 w2 = w, wg2 = wg;
  const __attribute__((address_space(4))) u64* ka = (const __attribute__((address_space(4))) u64*)__builtin_amdgcn_kernarg_segment_ptr();
  asm volatile("" : "+s"(w2), "+s"(wg2), "+s"(ka), "+s"(ws));
  const u32 l2 = __builtin_amdgcn_mbcnt_hi(~0u, __builtin_amdgcn_mbcnt_lo(~0u, 0u)), t2 = w2 * 64 + l2;
  const u32 kv2 = wg2 & 7u, b2 = wg2 >> 4, h2 = 2u * kv2 + ((wg2 >> 3) & 1u);
  u32 stv = pc[3];
  if (bad != 0) {
    asm volatile("s_waitcnt vmcnt(0)" ::: "memory");
    slow_path(lds, (int)t2, (gc16*)ka[0] + (size_t)b2 * SQ * DM, (gc16*)ka[3] + h2 * DH, (gc16*)ka[1] + (size_t)(b2 * HKV + kv2) * SK * DH,
              (gc16*)ka[2] + (size_t)(b2 * HKV + kv2) * SK * DH, (gc16*)ka[4] + (size_t)h2 * DH * DM,
              (gu16*)(ws + (size_t)(b2 * HQ + h2) * PART_BYTES));
    const u32 hw = h2 * 4 + w2;
    const u32 ca = 60 * hw + l2, cb = 3840 + 24 * hw + (l2 & 31u);
    const u32 oa = l2 < 60 ? out_off(ca) : 0x7ffffff0u, ob = l2 < 24 ? out_off(cb) : 0x7ffffff0u;
    u32x4 po = {l2 < 60 ? ca * 16 : 0x7ffffff0u, (l2 & 31u) < 24 ? cb * 16 + (l2 >> 5) * 8 * PART_BYTES : 0x7ffffff0u, oa, oa + 14336};
    u32x2 pq = {ob, ob + 14336};
    u32x2 pl = {(l2 & 15u) * 4, (l2 << 24) + b2 * FROW + h2 * 4};
    asm volatile(
    "s_and_b32 s75, s94, 0xffffffc0\n"
    "s_mov_b32 s65, 0\n"
    "s_mov_b32 s66, 0\n"
    "s_mov_b32 s70, 0\n"
    "s_mov_b32 s71, 0\n"
    "v_mov_b32 v174, s89\n"
    "v_add_u32 v174, 2, v174\n"
    "v_mov_b32 v176, 0\n"
    "v_mov_b32 v177, 0\n"
    "v_mov_b32 v178, 0\n"
    "v_mov_b32 v179, 0\n"
    "v_mov_b32 v180, 0\n"
    "v_mov_b32 v181, 0\n"
    "v_mov_b32 v182, 0\n"
    "v_mov_b32 v183, 0\n"
    "v_mbcnt_lo_u32_b32 v184, -1, 0\n"
    "v_mbcnt_hi_u32_b32 v184, -1, v184\n"
    "v_lshrrev_b32 v185, 4, v184\n"
    "v_bfe_u32 v186, v184, 2, 2\n"
    "v_and_b32 v187, 1, v184\n"
    "v_cmp_eq_u32 vcc, v185, v186\n"
    "v_lshlrev_b32 v187, 4, v187\n"
    "v_mov_b32 v185, 0x3f80\n"
    "v_lshlrev_b32 v187, v187, v185\n"
    "v_cndmask_b32 v187, 0, v187, vcc\n"
    "v_and_b32 v186, 2, v184\n"
    "v_cmp_eq_u32 vcc, 0, v186\n"
    "v_mov_b32 v194, 0\n"
    "v_mov_b32 v195, 0\n"
    "v_cndmask_b32 v196, 0, v187, vcc\n"
    "v_sub_u32 v197, v187, v196\n"
    "v_mov_b32 v198, 0\n"
    "v_mov_b32 v199, 0\n"
    "s_cmp_lg_u32 s83, 384\n"
    "s_waitcnt vmcnt(0)\n"
    "s_barrier\n"
    "s_cbranch_scc1 PL0S_%=\n"
    "buffer_store_dword v174, v229, s[96:99], 0 offen sc1\n"
    "PL0S_%=:\n"
    "PLOOPS_%=:\n"
    "buffer_load_dword v225, v228, s[96:99], s75 offen sc1\n"
    "s_add_u32 s71, s71, 1\n"
    "s_mov_b32 s76, s65\n"
    "s_waitcnt vmcnt(0)\n"
    "v_sub_u32 v184, v225, v207\n"
    "v_cmp_eq_u32 vcc, 2, v184\n"
    "v_add_u32 v185, -1, v184\n"
    "s_lshr_b32 s69, vcc_lo, 8\n"
    "s_and_b32 s69, s69, vcc_lo\n"
    "v_cmp_ge_u32 vcc, 1, v185\n"
    "s_and_b32 s69, s69, 0xff\n"
    "s_andn2_b32 s69, s69, s66\n"
    "s_and_b32 s68, vcc_lo, 0xffff\n"
    "s_andn2_b32 s68, s68, s65\n"
    "s_cmp_eq_u32 s68, 0\n"
    "s_cbranch_scc1 PG1ES_%=\n"
    "s_mov_b32 s77, 0\n"
    "s_bitcmp1_b32 s68, 0\n"
    "s_cbranch_scc0 PG10S_%=\n"
    "buffer_load_dwordx4 v[48:51], v168, s[40:43], s77 offen sc1\n"
    "PG10S_%=:\n"
    "s_mov_b32 s77, 86144\n"
    "s_bitcmp1_b32 s68, 1\n"
    "s_cbranch_scc0 PG11S_%=\n"
    "buffer_load_dwordx4 v[52:55], v168, s[40:43], s77 offen sc1\n"
    "PG11S_%=:\n"
    "s_mov_b32 s77, 172288\n"
    "s_bitcmp1_b32 s68, 2\n"
    "s_cbranch_scc0 PG12S_%=\n"
    "buffer_load_dwordx4 v[56:59], v168, s[40:43], s77 offen sc1\n"
    "PG12S_%=:\n"
    "s_mov_b32 s77, 258432\n"
    "s_bitcmp1_b32 s68, 3\n"
    "s_cbranch_scc0 PG13S_%=\n"
    "buffer_load_dwordx4 v[60:63], v168, s[40:43], s77 offen sc1\n"
    "PG13S_%=:\n"
    "s_mov_b32 s77, 344576\n"
    "s_bitcmp1_b32 s68, 4\n"
    "s_cbranch_scc0 PG14S_%=\n"
    "buffer_load_dwordx4 v[64:67], v168, s[40:43], s77 offen sc1\n"
    "PG14S_%=:\n"
    "s_mov_b32 s77, 430720\n"
    "s_bitcmp1_b32 s68, 5\n"
    "s_cbranch_scc0 PG15S_%=\n"
    "buffer_load_dwordx4 v[68:71], v168, s[40:43], s77 offen sc1\n"
    "PG15S_%=:\n"
    "s_mov_b32 s77, 516864\n"
    "s_bitcmp1_b32 s68, 6\n"
    "s_cbranch_scc0 PG16S_%=\n"
    "buffer_load_dwordx4 v[72:75], v168, s[40:43], s77 offen sc1\n"
    "PG16S_%=:\n"
    "s_mov_b32 s77, 603008\n"
    "s_bitcmp1_b32 s68, 7\n"
    "s_cbranch_scc0 PG17S_%=\n"
    "buffer_load_dwordx4 v[76:79], v168, s[40:43], s77 offen sc1\n"
    "PG17S_%=:\n"
    "s_mov_b32 s77, 689152\n"
    "s_bitcmp1_b32 s68, 8\n"
    "s_cbranch_scc0 PG18S_%=\n"
    "buffer_load_dwordx4 v[80:83], v168, s[40:43], s77 offen sc1\n"
    "PG18S_%=:\n"
    "s_mov_b32 s77, 775296\n"
    "s_bitcmp1_b32 s68, 9\n"
    "s_cbranch_scc0 PG19S_%=\n"
    "buffer_load_dwordx4 v[84:87], v168, s[40:43], s77 offen sc1\n"
    "PG19S_%=:\n"
    "s_mov_b32 s77, 861440\n"
    "s_bitcmp1_b32 s68, 10\n"
    "s_cbranch_scc0 PG110S_%=\n"
    "buffer_load_dwordx4 v[88:91], v168, s[40:43], s77 offen sc1\n"
    "PG110S_%=:\n"
    "s_mov_b32 s77, 947584\n"
    "s_bitcmp1_b32 s68, 11\n"
    "s_cbranch_scc0 PG111S_%=\n"
    "buffer_load_dwordx4 v[92:95], v168, s[40:43], s77 offen sc1\n"
    "PG111S_%=:\n"
    "s_mov_b32 s77, 1033728\n"
    "s_bitcmp1_b32 s68, 12\n"
    "s_cbranch_scc0 PG112S_%=\n"
    "buffer_load_dwordx4 v[96:99], v168, s[40:43], s77 offen sc1\n"
    "PG112S_%=:\n"
    "s_mov_b32 s77, 1119872\n"
    "s_bitcmp1_b32 s68, 13\n"
    "s_cbranch_scc0 PG113S_%=\n"
    "buffer_load_dwordx4 v[100:103], v168, s[40:43], s77 offen sc1\n"
    "PG113S_%=:\n"
    "s_mov_b32 s77, 1206016\n"
    "s_bitcmp1_b32 s68, 14\n"
    "s_cbranch_scc0 PG114S_%=\n"
    "buffer_load_dwordx4 v[104:107], v168, s[40:43], s77 offen sc1\n"
    "PG114S_%=:\n"
    "s_mov_b32 s77, 1292160\n"
    "s_bitcmp1_b32 s68, 15\n"
    "s_cbranch_scc0 PG115S_%=\n"
    "buffer_load_dwordx4 v[108:111], v168, s[40:43], s77 offen sc1\n"
    "PG115S_%=:\n"
    "PG1ES_%=:\n"
    "s_cmp_eq_u32 s69, 0\n"
    "s_cbranch_scc1 PG2ES_%=\n"
    "s_mov_b32 s77, 0\n"
    "s_bitcmp1_b32 s69, 0\n"
    "s_cbranch_scc0 PG20S_%=\n"
    "buffer_load_dwordx4 v[112:115], v169, s[40:43], s77 offen sc1\n"
    "PG20S_%=:\n"
    "s_mov_b32 s77, 86144\n"
    "s_bitcmp1_b32 s69, 1\n"
    "s_cbranch_scc0 PG21S_%=\n"
    "buffer_load_dwordx4 v[116:119], v169, s[40:43], s77 offen sc1\n"
    "PG21S_%=:\n"
    "s_mov_b32 s77, 172288\n"
    "s_bitcmp1_b32 s69, 2\n"
    "s_cbranch_scc0 PG22S_%=\n"
    "buffer_load_dwordx4 v[120:123], v169, s[40:43], s77 offen sc1\n"
    "PG22S_%=:\n"
    "s_mov_b32 s77, 258432\n"
    "s_bitcmp1_b32 s69, 3\n"
    "s_cbranch_scc0 PG23S_%=\n"
    "buffer_load_dwordx4 v[124:127], v169, s[40:43], s77 offen sc1\n"
    "PG23S_%=:\n"
    "s_mov_b32 s77, 344576\n"
    "s_bitcmp1_b32 s69, 4\n"
    "s_cbranch_scc0 PG24S_%=\n"
    "buffer_load_dwordx4 v[128:131], v169, s[40:43], s77 offen sc1\n"
    "PG24S_%=:\n"
    "s_mov_b32 s77, 430720\n"
    "s_bitcmp1_b32 s69, 5\n"
    "s_cbranch_scc0 PG25S_%=\n"
    "buffer_load_dwordx4 v[132:135], v169, s[40:43], s77 offen sc1\n"
    "PG25S_%=:\n"
    "s_mov_b32 s77, 516864\n"
    "s_bitcmp1_b32 s69, 6\n"
    "s_cbranch_scc0 PG26S_%=\n"
    "buffer_load_dwordx4 v[136:139], v169, s[40:43], s77 offen sc1\n"
    "PG26S_%=:\n"
    "s_mov_b32 s77, 603008\n"
    "s_bitcmp1_b32 s69, 7\n"
    "s_cbranch_scc0 PG27S_%=\n"
    "buffer_load_dwordx4 v[140:143], v169, s[40:43], s77 offen sc1\n"
    "PG27S_%=:\n"
    "PG2ES_%=:\n"
    "s_or_b32 s65, s65, s68\n"
    "s_or_b32 s66, s66, s69\n"
    "s_add_u32 s77, s76, 1\n"
    "s_xor_b32 s77, s77, s76\n"
    "s_lshr_b32 s77, s77, 1\n"
    "s_andn2_b32 s68, s77, s70\n"
    "s_mov_b32 s70, s77\n"
    "s_cmp_eq_u32 s68, 0\n"
    "s_cbranch_scc1 PQES_%=\n"
    "s_bitcmp1_b32 s68, 0\n"
    "s_cbranch_scc0 PQ0S_%=\n"
    "v_mfma_f32_16x16x32_bf16 v[176:179], v[196:199], v[48:51], v[176:179]\n"
    "v_mfma_f32_16x16x32_bf16 v[180:183], v[194:197], v[48:51], v[180:183]\n"
    "PQ0S_%=:\n"
    "s_bitcmp1_b32 s68, 1\n"
    "s_cbranch_scc0 PQ1S_%=\n"
    "v_mfma_f32_16x16x32_bf16 v[176:179], v[196:199], v[52:55], v[176:179]\n"
    "v_mfma_f32_16x16x32_bf16 v[180:183], v[194:197], v[52:55], v[180:183]\n"
    "PQ1S_%=:\n"
    "s_bitcmp1_b32 s68, 2\n"
    "s_cbranch_scc0 PQ2S_%=\n"
    "v_mfma_f32_16x16x32_bf16 v[176:179], v[196:199], v[56:59], v[176:179]\n"
    "v_mfma_f32_16x16x32_bf16 v[180:183], v[194:197], v[56:59], v[180:183]\n"
    "PQ2S_%=:\n"
    "s_bitcmp1_b32 s68, 3\n"
    "s_cbranch_scc0 PQ3S_%=\n"
    "v_mfma_f32_16x16x32_bf16 v[176:179], v[196:199], v[60:63], v[176:179]\n"
    "v_mfma_f32_16x16x32_bf16 v[180:183], v[194:197], v[60:63], v[180:183]\n"
    "PQ3S_%=:\n"
    "s_bitcmp1_b32 s68, 4\n"
    "s_cbranch_scc0 PQ4S_%=\n"
    "v_mfma_f32_16x16x32_bf16 v[176:179], v[196:199], v[64:67], v[176:179]\n"
    "v_mfma_f32_16x16x32_bf16 v[180:183], v[194:197], v[64:67], v[180:183]\n"
    "PQ4S_%=:\n"
    "s_bitcmp1_b32 s68, 5\n"
    "s_cbranch_scc0 PQ5S_%=\n"
    "v_mfma_f32_16x16x32_bf16 v[176:179], v[196:199], v[68:71], v[176:179]\n"
    "v_mfma_f32_16x16x32_bf16 v[180:183], v[194:197], v[68:71], v[180:183]\n"
    "PQ5S_%=:\n"
    "s_bitcmp1_b32 s68, 6\n"
    "s_cbranch_scc0 PQ6S_%=\n"
    "v_mfma_f32_16x16x32_bf16 v[176:179], v[196:199], v[72:75], v[176:179]\n"
    "v_mfma_f32_16x16x32_bf16 v[180:183], v[194:197], v[72:75], v[180:183]\n"
    "PQ6S_%=:\n"
    "s_bitcmp1_b32 s68, 7\n"
    "s_cbranch_scc0 PQ7S_%=\n"
    "v_mfma_f32_16x16x32_bf16 v[176:179], v[196:199], v[76:79], v[176:179]\n"
    "v_mfma_f32_16x16x32_bf16 v[180:183], v[194:197], v[76:79], v[180:183]\n"
    "PQ7S_%=:\n"
    "s_bitcmp1_b32 s68, 8\n"
    "s_cbranch_scc0 PQ8S_%=\n"
    "v_mfma_f32_16x16x32_bf16 v[176:179], v[196:199], v[80:83], v[176:179]\n"
    "v_mfma_f32_16x16x32_bf16 v[180:183], v[194:197], v[80:83], v[180:183]\n"
    "PQ8S_%=:\n"
    "s_bitcmp1_b32 s68, 9\n"
    "s_cbranch_scc0 PQ9S_%=\n"
    "v_mfma_f32_16x16x32_bf16 v[176:179], v[196:199], v[84:87], v[176:179]\n"
    "v_mfma_f32_16x16x32_bf16 v[180:183], v[194:197], v[84:87], v[180:183]\n"
    "PQ9S_%=:\n"
    "s_bitcmp1_b32 s68, 10\n"
    "s_cbranch_scc0 PQ10S_%=\n"
    "v_mfma_f32_16x16x32_bf16 v[176:179], v[196:199], v[88:91], v[176:179]\n"
    "v_mfma_f32_16x16x32_bf16 v[180:183], v[194:197], v[88:91], v[180:183]\n"
    "PQ10S_%=:\n"
    "s_bitcmp1_b32 s68, 11\n"
    "s_cbranch_scc0 PQ11S_%=\n"
    "v_mfma_f32_16x16x32_bf16 v[176:179], v[196:199], v[92:95], v[176:179]\n"
    "v_mfma_f32_16x16x32_bf16 v[180:183], v[194:197], v[92:95], v[180:183]\n"
    "PQ11S_%=:\n"
    "s_bitcmp1_b32 s68, 12\n"
    "s_cbranch_scc0 PQ12S_%=\n"
    "v_mfma_f32_16x16x32_bf16 v[176:179], v[196:199], v[96:99], v[176:179]\n"
    "v_mfma_f32_16x16x32_bf16 v[180:183], v[194:197], v[96:99], v[180:183]\n"
    "PQ12S_%=:\n"
    "s_bitcmp1_b32 s68, 13\n"
    "s_cbranch_scc0 PQ13S_%=\n"
    "v_mfma_f32_16x16x32_bf16 v[176:179], v[196:199], v[100:103], v[176:179]\n"
    "v_mfma_f32_16x16x32_bf16 v[180:183], v[194:197], v[100:103], v[180:183]\n"
    "PQ13S_%=:\n"
    "s_bitcmp1_b32 s68, 14\n"
    "s_cbranch_scc0 PQ14S_%=\n"
    "v_mfma_f32_16x16x32_bf16 v[176:179], v[196:199], v[104:107], v[176:179]\n"
    "v_mfma_f32_16x16x32_bf16 v[180:183], v[194:197], v[104:107], v[180:183]\n"
    "PQ14S_%=:\n"
    "s_bitcmp1_b32 s68, 15\n"
    "s_cbranch_scc0 PQ15S_%=\n"
    "v_mfma_f32_16x16x32_bf16 v[176:179], v[196:199], v[108:111], v[176:179]\n"
    "v_mfma_f32_16x16x32_bf16 v[180:183], v[194:197], v[108:111], v[180:183]\n"
    "PQ15S_%=:\n"
    "PQES_%=:\n"
    "s_lshl_b32 s77, s66, 16\n"
    "s_or_b32 s77, s77, s65\n"
    "s_cmp_eq_u32 s77, 0xffffff\n"
    "s_cbranch_scc1 PENDS_%=\n"
    "s_cmp_lt_u32 s71, 0x40000\n"
    "s_cbranch_scc1 PLOOPS_%=\n"
    "PENDS_%=:\n"
    "s_waitcnt vmcnt(0)\n"
    "s_add_u32 s77, s65, 1\n"
    "s_xor_b32 s77, s77, s65\n"
    "s_lshr_b32 s77, s77, 1\n"
    "s_andn2_b32 s68, s77, s70\n"
    "s_mov_b32 s70, s77\n"
    "s_cmp_eq_u32 s68, 0\n"
    "s_cbranch_scc1 PRES_%=\n"
    "s_bitcmp1_b32 s68, 0\n"
    "s_cbranch_scc0 PR0S_%=\n"
    "v_mfma_f32_16x16x32_bf16 v[176:179], v[196:199], v[48:51], v[176:179]\n"
    "v_mfma_f32_16x16x32_bf16 v[180:183], v[194:197], v[48:51], v[180:183]\n"
    "PR0S_%=:\n"
    "s_bitcmp1_b32 s68, 1\n"
    "s_cbranch_scc0 PR1S_%=\n"
    "v_mfma_f32_16x16x32_bf16 v[176:179], v[196:199], v[52:55], v[176:179]\n"
    "v_mfma_f32_16x16x32_bf16 v[180:183], v[194:197], v[52:55], v[180:183]\n"
    "PR1S_%=:\n"
    "s_bitcmp1_b32 s68, 2\n"
    "s_cbranch_scc0 PR2S_%=\n"
    "v_mfma_f32_16x16x32_bf16 v[176:179], v[196:199], v[56:59], v[176:179]\n"
    "v_mfma_f32_16x16x32_bf16 v[180:183], v[194:197], v[56:59], v[180:183]\n"
    "PR2S_%=:\n"
    "s_bitcmp1_b32 s68, 3\n"
    "s_cbranch_scc0 PR3S_%=\n"
    "v_mfma_f32_16x16x32_bf16 v[176:179], v[196:199], v[60:63], v[176:179]\n"
    "v_mfma_f32_16x16x32_bf16 v[180:183], v[194:197], v[60:63], v[180:183]\n"
    "PR3S_%=:\n"
    "s_bitcmp1_b32 s68, 4\n"
    "s_cbranch_scc0 PR4S_%=\n"
    "v_mfma_f32_16x16x32_bf16 v[176:179], v[196:199], v[64:67], v[176:179]\n"
    "v_mfma_f32_16x16x32_bf16 v[180:183], v[194:197], v[64:67], v[180:183]\n"
    "PR4S_%=:\n"
    "s_bitcmp1_b32 s68, 5\n"
    "s_cbranch_scc0 PR5S_%=\n"
    "v_mfma_f32_16x16x32_bf16 v[176:179], v[196:199], v[68:71], v[176:179]\n"
    "v_mfma_f32_16x16x32_bf16 v[180:183], v[194:197], v[68:71], v[180:183]\n"
    "PR5S_%=:\n"
    "s_bitcmp1_b32 s68, 6\n"
    "s_cbranch_scc0 PR6S_%=\n"
    "v_mfma_f32_16x16x32_bf16 v[176:179], v[196:199], v[72:75], v[176:179]\n"
    "v_mfma_f32_16x16x32_bf16 v[180:183], v[194:197], v[72:75], v[180:183]\n"
    "PR6S_%=:\n"
    "s_bitcmp1_b32 s68, 7\n"
    "s_cbranch_scc0 PR7S_%=\n"
    "v_mfma_f32_16x16x32_bf16 v[176:179], v[196:199], v[76:79], v[176:179]\n"
    "v_mfma_f32_16x16x32_bf16 v[180:183], v[194:197], v[76:79], v[180:183]\n"
    "PR7S_%=:\n"
    "s_bitcmp1_b32 s68, 8\n"
    "s_cbranch_scc0 PR8S_%=\n"
    "v_mfma_f32_16x16x32_bf16 v[176:179], v[196:199], v[80:83], v[176:179]\n"
    "v_mfma_f32_16x16x32_bf16 v[180:183], v[194:197], v[80:83], v[180:183]\n"
    "PR8S_%=:\n"
    "s_bitcmp1_b32 s68, 9\n"
    "s_cbranch_scc0 PR9S_%=\n"
    "v_mfma_f32_16x16x32_bf16 v[176:179], v[196:199], v[84:87], v[176:179]\n"
    "v_mfma_f32_16x16x32_bf16 v[180:183], v[194:197], v[84:87], v[180:183]\n"
    "PR9S_%=:\n"
    "s_bitcmp1_b32 s68, 10\n"
    "s_cbranch_scc0 PR10S_%=\n"
    "v_mfma_f32_16x16x32_bf16 v[176:179], v[196:199], v[88:91], v[176:179]\n"
    "v_mfma_f32_16x16x32_bf16 v[180:183], v[194:197], v[88:91], v[180:183]\n"
    "PR10S_%=:\n"
    "s_bitcmp1_b32 s68, 11\n"
    "s_cbranch_scc0 PR11S_%=\n"
    "v_mfma_f32_16x16x32_bf16 v[176:179], v[196:199], v[92:95], v[176:179]\n"
    "v_mfma_f32_16x16x32_bf16 v[180:183], v[194:197], v[92:95], v[180:183]\n"
    "PR11S_%=:\n"
    "s_bitcmp1_b32 s68, 12\n"
    "s_cbranch_scc0 PR12S_%=\n"
    "v_mfma_f32_16x16x32_bf16 v[176:179], v[196:199], v[96:99], v[176:179]\n"
    "v_mfma_f32_16x16x32_bf16 v[180:183], v[194:197], v[96:99], v[180:183]\n"
    "PR12S_%=:\n"
    "s_bitcmp1_b32 s68, 13\n"
    "s_cbranch_scc0 PR13S_%=\n"
    "v_mfma_f32_16x16x32_bf16 v[176:179], v[196:199], v[100:103], v[176:179]\n"
    "v_mfma_f32_16x16x32_bf16 v[180:183], v[194:197], v[100:103], v[180:183]\n"
    "PR13S_%=:\n"
    "s_bitcmp1_b32 s68, 14\n"
    "s_cbranch_scc0 PR14S_%=\n"
    "v_mfma_f32_16x16x32_bf16 v[176:179], v[196:199], v[104:107], v[176:179]\n"
    "v_mfma_f32_16x16x32_bf16 v[180:183], v[194:197], v[104:107], v[180:183]\n"
    "PR14S_%=:\n"
    "s_bitcmp1_b32 s68, 15\n"
    "s_cbranch_scc0 PR15S_%=\n"
    "v_mfma_f32_16x16x32_bf16 v[176:179], v[196:199], v[108:111], v[176:179]\n"
    "v_mfma_f32_16x16x32_bf16 v[180:183], v[194:197], v[108:111], v[180:183]\n"
    "PR15S_%=:\n"
    "PRES_%=:\n"
    "s_nop 7\n"
    "v_cvt_pk_bf16_f32 v184, v176, v177\n"
    "v_cvt_pk_bf16_f32 v185, v178, v179\n"
    "v_cvt_pk_bf16_f32 v186, v180, v181\n"
    "v_cvt_pk_bf16_f32 v187, v182, v183\n"
    "buffer_store_dwordx2 v[184:185], v170, s[44:47], 0 offen\n"
    "buffer_store_dwordx2 v[186:187], v171, s[44:47], 0 offen\n"
    "v_mfma_f32_16x16x32_bf16 v[176:179], v[196:199], v[112:115], 0\n"
    "v_mfma_f32_16x16x32_bf16 v[180:183], v[194:197], v[112:115], 0\n"
    "v_mfma_f32_16x16x32_bf16 v[176:179], v[196:199], v[116:119], v[176:179]\n"
    "v_mfma_f32_16x16x32_bf16 v[180:183], v[194:197], v[116:119], v[180:183]\n"
    "v_mfma_f32_16x16x32_bf16 v[176:179], v[196:199], v[120:123], v[176:179]\n"
    "v_mfma_f32_16x16x32_bf16 v[180:183], v[194:197], v[120:123], v[180:183]\n"
    "v_mfma_f32_16x16x32_bf16 v[176:179], v[196:199], v[124:127], v[176:179]\n"
    "v_mfma_f32_16x16x32_bf16 v[180:183], v[194:197], v[124:127], v[180:183]\n"
    "v_mfma_f32_16x16x32_bf16 v[176:179], v[196:199], v[128:131], v[176:179]\n"
    "v_mfma_f32_16x16x32_bf16 v[180:183], v[194:197], v[128:131], v[180:183]\n"
    "v_mfma_f32_16x16x32_bf16 v[176:179], v[196:199], v[132:135], v[176:179]\n"
    "v_mfma_f32_16x16x32_bf16 v[180:183], v[194:197], v[132:135], v[180:183]\n"
    "v_mfma_f32_16x16x32_bf16 v[176:179], v[196:199], v[136:139], v[176:179]\n"
    "v_mfma_f32_16x16x32_bf16 v[180:183], v[194:197], v[136:139], v[180:183]\n"
    "v_mfma_f32_16x16x32_bf16 v[176:179], v[196:199], v[140:143], v[176:179]\n"
    "v_mfma_f32_16x16x32_bf16 v[180:183], v[194:197], v[140:143], v[180:183]\n"
    "s_nop 7\n"
    "v_mov_b32 v188, v176\n"
    "v_mov_b32 v189, v177\n"
    "v_mov_b32 v190, v178\n"
    "v_mov_b32 v191, v179\n"
    "v_mov_b32 v192, v180\n"
    "v_mov_b32 v193, v181\n"
    "v_mov_b32 v194, v182\n"
    "v_mov_b32 v195, v183\n"
    "s_nop 1\n"
    "v_permlane32_swap_b32 v176, v188\n"
    "v_permlane32_swap_b32 v177, v189\n"
    "v_permlane32_swap_b32 v178, v190\n"
    "v_permlane32_swap_b32 v179, v191\n"
    "v_permlane32_swap_b32 v180, v192\n"
    "v_permlane32_swap_b32 v181, v193\n"
    "v_permlane32_swap_b32 v182, v194\n"
    "v_permlane32_swap_b32 v183, v195\n"
    "s_nop 1\n"
    "v_add_f32 v176, v176, v188\n"
    "v_add_f32 v177, v177, v189\n"
    "v_add_f32 v178, v178, v190\n"
    "v_add_f32 v179, v179, v191\n"
    "v_add_f32 v180, v180, v192\n"
    "v_add_f32 v181, v181, v193\n"
    "v_add_f32 v182, v182, v194\n"
    "v_add_f32 v183, v183, v195\n"
    "s_nop 0\n"
    "s_nop 7\n"
    "v_cvt_pk_bf16_f32 v184, v176, v177\n"
    "v_cvt_pk_bf16_f32 v185, v178, v179\n"
    "v_cvt_pk_bf16_f32 v186, v180, v181\n"
    "v_cvt_pk_bf16_f32 v187, v182, v183\n"
    "buffer_store_dwordx2 v[184:185], v172, s[44:47], 0 offen\n"
    "buffer_store_dwordx2 v[186:187], v173, s[44:47], 0 offen\n"
      : "+{v227}"(stv)
      : "{v207}"(vn), "{v[228:229]}"(pl), "{v[168:171]}"(po), "{v[172:173]}"(pq), "{s[40:43]}"(srd(ws + (size_t)b2 * HQ * PART_BYTES, HQ * PART_BYTES)),
        "{s[44:47]}"(srd((u16*)ka[6] + (size_t)b2 * SQ * DM, SQ * DM * 2)), "{s[96:99]}"(srd(ws + FLAG_OFF, FLAG_BYTES)), "{s89}"(sn),
        "{s94}"(b2 * FROW + h2 * 4), "{s83}"(w2 * 128)
      : "v48","v49","v50","v51","v52","v53","v54","v55","v56","v57","v58","v59","v60","v61","v62","v63","v64","v65","v66","v67","v68","v69","v70","v71","v72","v73","v74","v75","v76","v77","v78","v79","v80","v81","v82","v83","v84","v85","v86","v87","v88","v89","v90","v91","v92","v93","v94","v95","v96","v97","v98","v99","v100","v101","v102","v103","v104","v105","v106","v107","v108","v109","v110","v111","v112","v113","v114","v115","v116","v117","v118","v119","v120","v121","v122","v123","v124","v125","v126","v127","v128","v129","v130","v131","v132","v133","v134","v135","v136","v137","v138","v139","v140","v141","v142","v143","v174","v175","v176","v177","v178","v179","v180","v181","v182","v183","v184","v185","v186","v187","v188","v189","v190","v191","v192","v193","v194","v195","v196","v197","v198","v199","v225","s64","s65","s66","s67","s68","s69","s70","s71","s72","s73","s74","s75","s76","s77","s78","s79","s80","s81","s92","s93","vcc","scc","memory");
  }
}

extern "C" void kernel_launch(void* const* d_in, const int* in_sizes, int n_in, void* d_out, int out_size, void* d_ws, size_t ws_size,
                              hipStream_t stream) {
  (void)in_sizes; (void)n_in; (void)out_size; (void)ws_size;
  (void)hipFuncSetAttribute((const void*)gqa_fused, hipFuncAttributeMaxDynamicSharedMemorySize, (int)LDS_BYTES);
  gqa_fused<<<256, 256, LDS_BYTES, stream>>>((const u16*)d_in[0], (const u16*)d_in[1], (const u16*)d_in[2], (const u16*)d_in[3],
                                             (const u16*)d_in[4], (char*)d_ws, (u16*)d_out);
}
```
